```python
import torch

def _softmax(x, axis=-1):
    return torch.softmax(x, dim=axis)
def _gelu(x):
    return torch.nn.functional.gelu(x, approximate="tanh")
def _silu(x):
    return torch.nn.functional.silu(x)
def _sigmoid(x):
    return torch.sigmoid(x)
def _layernorm(x, axis=-1, eps=1e-5):
    m = x.mean(dim=axis, keepdim=True)
    v = x.var(dim=axis, keepdim=True, unbiased=False)
    return (x - m) / torch.sqrt(v + eps)
def _scatter_add(tgt, idx, src, axis=0):
    return tgt.clone().index_add_(axis, idx, src)
def _segment_sum(src, seg, n):
    out = torch.zeros((n,) + src.shape[1:], dtype=src.dtype)
    return out.index_add_(0, seg, src)
def _pad(x, pw):
    flat = tuple(v for pair in reversed(pw) for v in pair)
    return torch.nn.functional.pad(x, flat)

kernel_name = 'interleave_layers__spectral_gfnet__softmax_full_mha__prompt'

def setup_inputs():

    _w = (lambda a: (lambda t: t.to(torch.bfloat16) if t.is_floating_point() else t)(torch.as_tensor(a)))
    def _A_setup():
        B, S, D = 1, 2048, 576
        Df = D // 2 + 1
        return {
            "x": _w((lambda *s: torch.randn(*s, dtype=torch.bfloat16) * 0.05)(B, S, D)),
            "w_r": _w((lambda *s: torch.randn(*s, dtype=torch.bfloat16) * 1.6)(S, Df) * 0.02),
            "w_i": _w((lambda *s: torch.randn(*s, dtype=torch.bfloat16) * 1.6)(S, Df) * 0.02),
        }
    _A_in = _A_setup()

    def _B_setup():
        B, S, D = 1, 2048, 576
        return {
            "x": _w((lambda *s: torch.randn(*s, dtype=torch.bfloat16) * 0.05)(B, S, D)),
            "wqkv": _w((lambda *s: torch.randn(*s, dtype=torch.bfloat16) * 0.05)(D, 3 * D)),
            "wo": _w((lambda *s: torch.randn(*s, dtype=torch.bfloat16) * 0.05)(D, D)),
        }
    _B_in = _B_setup()

    return {
        "A_x": _A_in["x"],
        "A_w_r": _A_in["w_r"],
        "A_w_i": _A_in["w_i"],
        "B_x": _B_in["x"],
        "B_wqkv": _B_in["wqkv"],
        "B_wo": _B_in["wo"],
    }

def reference(A_x, A_w_r, A_w_i, B_x, B_wqkv, B_wo):
    def _A_ref(x, w_r, w_i):
        B, S, D = x.shape
        X = (lambda x, axes=(-2,-1): torch.fft.rfft2(x.float(), dim=axes))(x, axes=(-2, -1))
        W = w_r + 1j * w_i
        X = X * W
        y = (lambda x, s, axes=(-2,-1): torch.fft.irfft2(x, s=s, dim=axes).to(torch.bfloat16))(X, (S, D), axes=(-2, -1))
        return y

    def _B_ref(x, wqkv, wo):
        B, S, D = x.shape
        H = 32; Dh = D // H
        qkv = (lambda x, *d: x.permute(*d))((x @ wqkv).reshape(B, S, 3, H, Dh), 2, 0, 3, 1, 4)
        q, k, v = qkv[0], qkv[1], qkv[2]
        scale = Dh ** -0.5
        scores = torch.einsum("bhqd,bhkd->bhqk", q, k) * scale
        p = _softmax(scores, axis=-1)
        o = torch.einsum("bhqk,bhkd->bhqd", p, v)
        return (lambda x, *d: x.permute(*d))(o, 0, 2, 1, 3).reshape(B, S, D) @ wo

    x = A_x
    x = x + _A_ref(_layernorm(x, axis=-1), A_w_r, A_w_i)
    x = x + _B_ref(_layernorm(x, axis=-1), B_wqkv, B_wo)
    x = x + _A_ref(_layernorm(x, axis=-1), A_w_r, A_w_i)
    x = x + _B_ref(_layernorm(x, axis=-1), B_wqkv, B_wo)
    return x
```

```cpp
#include <hip/hip_runtime.h>
#include <hip/hip_bf16.h>
#include <stdint.h>
#include <utility>

typedef __attribute__((ext_vector_type(8))) __bf16 bf16x8;
typedef __attribute__((ext_vector_type(2))) __bf16 bf16x2;
typedef __attribute__((ext_vector_type(2))) float f32x2;
typedef __attribute__((ext_vector_type(4))) float f32x4;
typedef __attribute__((ext_vector_type(16))) float f32x16;
typedef __attribute__((ext_vector_type(4))) unsigned u32x4;
typedef __attribute__((ext_vector_type(2))) unsigned u32x2;
typedef unsigned short u16;
#define LAS __attribute__((address_space(3)))

constexpr int S = 2048, D = 576, NH = 32;

namespace tbl {
constexpr double PI = 3.14159265358979323846264338327950288;
constexpr double ccos(int m) {
    if (m > 288) m = 576 - m;
    double sg = 1.0;
    if (m > 144) { m = 288 - m; sg = -1.0; }
    if (m == 144) return 0.0;
    const double x = 2.0 * PI * m / 576.0, x2 = x * x;
    double term = 1.0, sum = 1.0;
    for (int i = 1; i < 16; ++i) { term *= -x2 / ((2 * i - 1) * (2 * i)); sum += term; }
    return sg * sum;
}
constexpr u16 tobf(double v) {
    const unsigned u = __builtin_bit_cast(unsigned, (float)v);
    return (u16)((u + 0x7FFFu + ((u >> 16) & 1u)) >> 16);
}
struct Base { u16 c[576]; };
constexpr Base mkbase() { Base b{}; for (int m = 0; m < 576; ++m) b.c[m] = tobf(0.05892556509887896 * ccos(m)); return b; }
constexpr Base base = mkbase();
constexpr u16 DCV = tobf(1.0 / 24.0), NDCV = tobf(-1.0 / 24.0);
constexpr u16 Fval(int n, int c) {
    const int j = c >> 1;
    if (j == 0) return (c == 0 || !(n & 1)) ? DCV : NDCV;
    return base.c[(j * n + ((c & 1) ? 144 : 0)) % 576];
}
constexpr int cl(int T, int p) { return T < 4 ? 32 * (T >> 1) + 8 * (p >> 2) + 4 * (T & 1) + (p & 3) : 64 + p; }
struct Chunk { u16 v[4608]; };
template <int I> constexpr Chunk mkF1() {
    Chunk c{}; const int a = I / 9;
    for (int ss = 0; ss < 2; ++ss) { const int s = (I % 9) * 2 + ss; int o = ss * 2304;
        for (int T = 0; T < 5; ++T) for (int g = 0; g < 4; ++g) for (int p = 0; p < (T < 4 ? 16 : 8); ++p) for (int e = 0; e < 8; ++e)
            c.v[o++] = Fval(32 * s + 8 * g + e, 72 * a + cl(T, p)); }
    return c;
}
template <int I> constexpr Chunk mkF2() {
    Chunk c{}; int o = 0;
    for (int q = 0; q < 16; ++q) { const int hs = I * 16 + q, h = hs / 36, s = hs % 36;
        for (int hh = 0; hh < 2; ++hh) for (int p = 0; p < 18; ++p) {
            const int n = p < 16 ? 16 * h + 8 * ((p >> 2) & 1) + (p & 3) + 4 * (p >> 3) : 512 + 2 * h + (p - 16);
            for (int e = 0; e < 8; ++e) c.v[o++] = Fval(n, 16 * s + 8 * hh + e); } }
    return c;
}
template <int I> constexpr Chunk f1c = mkF1<I>();
template <int I> constexpr Chunk f2c = mkF2<I>();
struct Table { Chunk c[72]; };
template <int... I> constexpr Table mkT1(std::integer_sequence<int, I...>) { return Table{{f1c<I>...}}; }
template <int... I> constexpr Table mkT2(std::integer_sequence<int, I...>) { return Table{{f2c<I>...}}; }
}
__device__ __attribute__((aligned(256))) const tbl::Table F1G = tbl::mkT1(std::make_integer_sequence<int, 72>{});
__device__ __attribute__((aligned(256))) const tbl::Table F2H = tbl::mkT2(std::make_integer_sequence<int, 72>{});

constexpr size_t CELLS = (size_t)72 * S * 16;
constexpr size_t GIMG = 82944;
constexpr size_t HIMG = 62208;
constexpr size_t KVCH = 18432;
constexpr size_t al(size_t x) { return (x + 4095) & ~(size_t)4095; }
constexpr size_t O_XH0 = 0, O_XH1 = O_XH0 + CELLS, O_XH2 = O_XH1 + CELLS, O_XH3 = O_XH2 + CELLS;
constexpr size_t O_OA = O_XH3 + CELLS, O_OB = O_OA + CELLS, O_X3 = O_OB + CELLS;
constexpr size_t O_WT = O_X3 + CELLS;
constexpr size_t O_WQT = O_WT + (size_t)256 * 288 * 32;
constexpr size_t O_WOC = O_WQT + (size_t)72 * 1728 * 16;
constexpr size_t O_WO2 = O_WOC + al(8 * GIMG + 4096);
constexpr size_t O_WOH = O_WO2 + al(8 * GIMG + 4096);
constexpr size_t O_WQH = O_WOH + al(8 * GIMG + 4096);
constexpr size_t O_KVA = O_WQH + al(32 * HIMG + 4096), O_KVB = O_KVA + 256 * KVCH;
constexpr size_t O_Q1A = O_KVB + 256 * KVCH, O_Q1B = O_Q1A + (size_t)32 * 64 * 1024;
constexpr size_t O_Q2A = O_Q1B + (size_t)32 * 64 * 1024, O_Q2B = O_Q2A + (size_t)32 * S * 4;
constexpr size_t O_SS0 = O_Q2B + (size_t)32 * S * 4, O_SS2 = O_SS0 + (size_t)8 * S * 4;

__device__ __forceinline__ unsigned pk(float a, float b) { f32x2 t = {a, b}; return __builtin_bit_cast(unsigned, __builtin_convertvector(t, bf16x2)); }
__device__ __forceinline__ float blo(unsigned u) { return __uint_as_float(u << 16); }
__device__ __forceinline__ float bhi(unsigned u) { return __uint_as_float(u & 0xFFFF0000u); }
__device__ __forceinline__ float sq2(unsigned v, float q) { const bf16x2 t = __builtin_bit_cast(bf16x2, v); return __builtin_amdgcn_fdot2_f32_bf16(t, t, q, false); }
__device__ __forceinline__ bf16x8 asb(u32x4 v) { return __builtin_bit_cast(bf16x8, v); }
__device__ __forceinline__ void glds16(const void* g, unsigned lds_base) {
    unsigned sv;
    asm volatile("s_mov_b32 %0, m0\n\ts_mov_b32 m0, %2\n\ts_nop 0\n\tglobal_load_lds_dwordx4 %1, off\n\ts_mov_b32 m0, %0" : "=&s"(sv) : "v"(g), "s"(lds_base) : "memory");
}
#define WAIT_BAR(N) asm volatile("s_waitcnt vmcnt(" #N ") lgkmcnt(0)\n\ts_barrier" ::: "memory")
#define MF16(a, b, c) __builtin_amdgcn_mfma_f32_16x16x32_bf16(a, b, c, 0, 0, 0)
#define MF32(a, b, c) __builtin_amdgcn_mfma_f32_32x32x16_bf16(a, b, c, 0, 0, 0)
#define SB() __builtin_amdgcn_sched_barrier(0)
__device__ __forceinline__ u32x4 lds128(const char* p) { return *(const LAS u32x4*)(p); }
__device__ __forceinline__ int clT(int T, int p) { return T < 4 ? 32 * (T >> 1) + 8 * (p >> 2) + 4 * (T & 1) + (p & 3) : 64 + p; }
__device__ __forceinline__ unsigned gimg_off(int kb, int c) {
    const int a = c / 72, x = c - 72 * a, s = kb >> 2, g = kb & 3;
    unsigned o;
    if (x < 64) { const int T = 2 * (x >> 5) + ((x >> 2) & 1), p = 4 * ((x >> 3) & 3) + (x & 3); o = T * 1024 + g * 256 + p * 16; }
    else o = 4096 + g * 128 + (x - 64) * 16;
    return a * (unsigned)GIMG + s * 4608 + o;
}
__device__ __forceinline__ int ofeat(int kb, int e) {
    return kb < 64 ? 18 * (kb >> 1) + (e & 3) + 8 * (e >> 2) + 4 * (kb & 1) : 18 * (4 * (kb - 64) + (e >> 1)) + 16 + (e & 1);
}

__device__ __forceinline__ void gemm72_issue(const char* cimg, char* lds, int wid, int lane) {
    const unsigned l0 = (unsigned)(uintptr_t)lds + wid * 1024;
    const char* src = cimg + wid * 1024 + lane * 16;
#pragma unroll
    for (int m = 0; m < 21; ++m) glds16(src + m * 4096, __builtin_amdgcn_readfirstlane(l0 + m * 4096));
}
__device__ __forceinline__ void gemm72_steps(const char* lds, int lane, const u32x4 (&rf)[18], f32x4 (&acc)[5], int s0) {
    const char* p = lds + lane * 16;
    const char* p4 = lds + 4096 + (lane >> 4) * 128 + (lane & 7) * 16;
#pragma unroll
    for (int ss = 0; ss < 6; ++ss) {
        const int s = s0 + ss;
#pragma unroll
        for (int T = 0; T < 4; ++T) acc[T] = MF16(asb(lds128(p + s * 4608 + T * 1024)), asb(rf[s]), acc[T]);
        acc[4] = MF16(asb(lds128(p4 + s * 4608)), asb(rf[s]), acc[4]);
    }
}
__device__ __forceinline__ void gemm72_run(const char* lds, int lane, const u32x4 (&rf)[18], f32x4 (&acc)[5]) {
    WAIT_BAR(14); gemm72_steps(lds, lane, rf, acc, 0);
    WAIT_BAR(7);  gemm72_steps(lds, lane, rf, acc, 6);
    WAIT_BAR(0);  gemm72_steps(lds, lane, rf, acc, 12);
}

__device__ __forceinline__ void store_xh(f32x4 (&acc)[5], char* xh, float* ssp, int a, int r, int g) {
    float q = 0.f;
#pragma unroll
    for (int u = 0; u < 2; ++u) {
        u32x4 o = {pk(acc[2 * u][0], acc[2 * u][1]), pk(acc[2 * u][2], acc[2 * u][3]), pk(acc[2 * u + 1][0], acc[2 * u + 1][1]), pk(acc[2 * u + 1][2], acc[2 * u + 1][3])};
        *(u32x4*)(xh + ((size_t)(9 * a + 4 * u + g) * S + r) * 16) = o;
        if (u == 0 && a == 0 && g == 0) o[0] &= 0xFFFF0000u;
#pragma unroll
        for (int i = 0; i < 4; ++i) q = sq2(o[i], q);
    }
    if (g < 2) {
        u32x2 o = {pk(acc[4][0], acc[4][1]), pk(acc[4][2], acc[4][3])};
        *(u32x2*)(xh + ((size_t)(9 * a + 8) * S + r) * 16 + g * 8) = o;
        q = sq2(o[0], q);
        q = sq2(o[1], q);
    }
    q += __shfl_xor(q, 16); q += __shfl_xor(q, 32);
    if (g == 0) ssp[a * S + r] = q;
}

__global__ __launch_bounds__(512) void k_in(const u16* __restrict__ x, const u16* __restrict__ wr, const u16* __restrict__ wi,
                                            const u16* __restrict__ wqkv, const u16* __restrict__ wo, char* __restrict__ ws) {
    __shared__ __attribute__((aligned(16))) char lds[86016];
    const int tid = threadIdx.x, lane = tid & 63, wid = __builtin_amdgcn_readfirstlane(tid >> 6), bx = blockIdx.x;
    if (wid < 4) {
        const int idx = bx >> 3, rb = (bx & 7) * 4 + (idx >> 3), a = idx & 7, p = lane & 15, g = lane >> 4, r = rb * 64 + wid * 16 + p;
        u32x4 rf[18];
#pragma unroll
        for (int s = 0; s < 18; ++s) rf[s] = *(const u32x4*)(x + (size_t)r * D + 32 * s + 8 * g);
        const char* img = (const char*)&F1G + a * GIMG;
        {
            const unsigned l0 = (unsigned)(uintptr_t)lds + wid * 1024;
#pragma unroll
            for (int m = 0; m < 21; ++m) { const int i = (m * 4 + wid) < 81 ? (m * 4 + wid) : 80; glds16(img + i * 1024 + lane * 16, __builtin_amdgcn_readfirstlane(l0 + m * 4096)); }
        }
        f32x4 acc[5] = {};
        gemm72_run(lds, lane, rf, acc);
        store_xh(acc, ws + O_XH0, (float*)(ws + O_SS0), a, r, g);
    } else {
        const int t = tid - 256;
        const int k1 = 16 * (bx >> 6) + (bx & 15), gq = (bx >> 4) & 3;
        u16 a0[8], b0[8], a1[8], b1[8];
        const int c0 = t == 0 ? 288 : t, c1 = 256 + (t & 31);
#pragma unroll
        for (int i = 0; i < 8; ++i) { const int k = k1 + 64 * (16 * (i >> 2) + 4 * gq + (i & 3));
            a0[i] = wr[k * 289 + c0]; b0[i] = wi[k * 289 + c0]; a1[i] = wr[k * 289 + c1]; b1[i] = wi[k * 289 + c1]; }
        const int cq = bx * 243 + (t < 243 ? t : 0), kbq = cq / 864, jq = (cq - kbq * 864) * 2;
        unsigned wq[8];
#pragma unroll
        for (int e = 0; e < 8; ++e) wq[e] = *(const unsigned*)(wqkv + (size_t)(8 * kbq + e) * 1728 + jq);
        const int co = bx * 162 + (t < 162 ? t : 0), qo = co / 72, kbo = co - qo * 72;
        const u32x4 woc = *(const u32x4*)(wo + (size_t)ofeat(qo >> 3, qo & 7) * D + 8 * kbo);
        const int kb4 = co / 576, n4 = co - kb4 * 576;
        u16 w4[8];
#pragma unroll
        for (int e = 0; e < 8; ++e) w4[e] = wo[(size_t)ofeat(kb4, e) * D + n4];
        __builtin_amdgcn_s_barrier(); __builtin_amdgcn_s_barrier(); __builtin_amdgcn_s_barrier();
        u32x4 o0, o1;
#pragma unroll
        for (int i = 0; i < 4; ++i) { o0[i] = a0[i] | ((unsigned)b0[i] << 16); o1[i] = a0[i + 4] | ((unsigned)b0[i + 4] << 16); }
        char* wt = ws + O_WT + ((size_t)bx * 288 + t) * 32;
        *(u32x4*)wt = o0; *(u32x4*)(wt + 16) = o1;
        if (t < 32) {
#pragma unroll
            for (int i = 0; i < 4; ++i) { o0[i] = a1[i] | ((unsigned)b1[i] << 16); o1[i] = a1[i + 4] | ((unsigned)b1[i + 4] << 16); }
            *(u32x4*)(wt + 256 * 32) = o0; *(u32x4*)(wt + 256 * 32 + 16) = o1;
        }
        if (t < 243) {
#pragma unroll
            for (int i = 0; i < 4; ++i) { o0[i] = (wq[2 * i] & 0xFFFFu) | (wq[2 * i + 1] << 16); o1[i] = (wq[2 * i] >> 16) | (wq[2 * i + 1] & 0xFFFF0000u); }
            char* d = ws + O_WQT + ((size_t)kbq * 1728 + jq) * 16;
            *(u32x4*)d = o0; *(u32x4*)(d + 16) = o1;
        }
        if (t < 162) {
            *(u32x4*)(ws + O_WOC + gimg_off(kbo, qo)) = woc;
#pragma unroll
            for (int i = 0; i < 4; ++i) o0[i] = w4[2 * i] | ((unsigned)w4[2 * i + 1] << 16);
            *(u32x4*)(ws + O_WO2 + gimg_off(kb4, n4)) = o0;
        }
    }
}

__device__ __forceinline__ unsigned himg_off(int kb, int j) {
    const int which = j / 576, rem = j - which * 576, h = rem / 18, d = rem - h * 18, s = kb >> 1, hh = kb & 1;
    unsigned o;
    if (which < 2 && d < 16) o = hh * 512 + (16 * which + (d & 3) + 8 * ((d >> 2) & 1) + 4 * (d >> 3)) * 16;
    else o = 1024 + hh * 352 + (which == 2 ? d : 2 + 2 * which + d) * 16;
    return h * (unsigned)HIMG + s * 1728 + o;
}
__global__ __launch_bounds__(256) void k_wprep(char* __restrict__ ws) {
    __shared__ __attribute__((aligned(16))) char lds[86016];
    const int tid = threadIdx.x, lane = tid & 63, wid = __builtin_amdgcn_readfirstlane(tid >> 6), bx = blockIdx.x, p = lane & 15, g = lane >> 4;
    const int a = bx & 7, tl = bx >> 3;
    u32x4 rf[18]; f32x4 acc[5] = {};
    if (tl < 27) {
        const int j = tl * 64 + wid * 16 + p;
#pragma unroll
        for (int s = 0; s < 18; ++s) rf[s] = *(const u32x4*)(ws + O_WQT + ((size_t)(4 * s + g) * 1728 + j) * 16);
        const char* img = (const char*)&F1G + a * GIMG;
        const unsigned l0 = (unsigned)(uintptr_t)lds + wid * 1024;
#pragma unroll
        for (int m = 0; m < 21; ++m) { const int i = (m * 4 + wid) < 81 ? (m * 4 + wid) : 80; glds16(img + i * 1024 + lane * 16, __builtin_amdgcn_readfirstlane(l0 + m * 4096)); }
        gemm72_run(lds, lane, rf, acc);
        if (a == 0 && g == 0) acc[0][0] = 0.f;
#pragma unroll
        for (int u = 0; u < 2; ++u) {
            u32x4 o = {pk(acc[2 * u][0], acc[2 * u][1]), pk(acc[2 * u][2], acc[2 * u][3]), pk(acc[2 * u + 1][0], acc[2 * u + 1][1]), pk(acc[2 * u + 1][2], acc[2 * u + 1][3])};
            *(u32x4*)(ws + O_WQH + himg_off(9 * a + 4 * u + g, j)) = o;
        }
        if (g < 2) { u32x2 o = {pk(acc[4][0], acc[4][1]), pk(acc[4][2], acc[4][3])}; *(u32x2*)(ws + O_WQH + himg_off(9 * a + 8, j) + g * 8) = o; }
    } else {
        const int c = (tl - 27) * 64 + wid * 16 + p;
#pragma unroll
        for (int s = 0; s < 18; ++s) rf[s] = *(const u32x4*)((const char*)&F1G + gimg_off(4 * s + g, c));
        gemm72_issue(ws + O_WOC + a * GIMG, lds, wid, lane);
        gemm72_run(lds, lane, rf, acc);
#pragma unroll
        for (int u = 0; u < 2; ++u) {
            u32x4 o = {pk(acc[2 * u][0], acc[2 * u][1]), pk(acc[2 * u][2], acc[2 * u][3]), pk(acc[2 * u + 1][0], acc[2 * u + 1][1]), pk(acc[2 * u + 1][2], acc[2 * u + 1][3])};
            *(u32x4*)(ws + O_WOH + gimg_off(9 * a + 4 * u + g, c)) = o;
        }
        if (g < 2) { u32x2 o = {pk(acc[4][0], acc[4][1]), pk(acc[4][2], acc[4][3])}; *(u32x2*)(ws + O_WOH + gimg_off(9 * a + 8, c) + g * 8) = o; }
    }
}

__device__ __forceinline__ u32x4 neg8(u32x4 v) { return v ^ 0x80008000u; }
__global__ __launch_bounds__(256) void k_fft(const char* __restrict__ xin, char* __restrict__ xout, const float* __restrict__ ssp, const char* __restrict__ wt) {
    __shared__ __attribute__((aligned(16))) char lds[16384];
    const int tid = threadIdx.x, lane = tid & 63, w = __builtin_amdgcn_readfirstlane(tid >> 6), col = lane & 15, g = lane >> 4;
    const int bx = blockIdx.x, kb = (bx & 7) * 9 + (bx >> 5), j = kb * 4 + ((bx >> 3) & 3);
    const size_t cell0 = (size_t)kb * S * 16 + (j & 3) * 4;
    unsigned xo[8]; float ss[8];
#pragma unroll
    for (int i = 0; i < 8; ++i) { const int n = 32 * (16 * w + 4 * g + (i & 3)) + 16 * (i >> 2) + col;
        xo[i] = *(const unsigned*)(xin + cell0 + (size_t)n * 16);
        float q = 0.f;
#pragma unroll
        for (int a = 0; a < 8; ++a) q += ssp[a * S + n];
        ss[i] = q; }
    const u32x4 w0 = *(const u32x4*)(wt + ((size_t)tid * 288 + j) * 32), w1 = *(const u32x4*)(wt + ((size_t)tid * 288 + j) * 32 + 16);
    u32x4 f64r[2], f64i[2], f32r[2], f32i[2];
    float t1c[8], t1s[8], t2c[8], t2s[8];
    {
        const float k1 = (float)(16 * w + col);
#pragma unroll
        for (int s = 0; s < 2; ++s)
#pragma unroll
            for (int q = 0; q < 4; ++q) {
                const int j0 = 2 * q;
                const float n0 = (float)(32 * s + 16 * (j0 >> 2) + 4 * g + (j0 & 3));
                const float a0 = n0 * k1 * (1.f / 64.f), a1 = (n0 + 1.f) * k1 * (1.f / 64.f);
                f64r[s][q] = pk(__builtin_amdgcn_cosf(a0), __builtin_amdgcn_cosf(a1));
                f64i[s][q] = pk(-__builtin_amdgcn_sinf(a0), -__builtin_amdgcn_sinf(a1));
                const float m0 = (float)(16 * (j0 >> 2) + 4 * g + (j0 & 3)), k2 = (float)(16 * s + col);
                const float b0 = m0 * k2 * (1.f / 32.f), b1 = (m0 + 1.f) * k2 * (1.f / 32.f);
                f32r[s][q] = pk(__builtin_amdgcn_cosf(b0), __builtin_amdgcn_cosf(b1));
                f32i[s][q] = pk(-__builtin_amdgcn_sinf(b0), -__builtin_amdgcn_sinf(b1));
            }
#pragma unroll
        for (int i = 0; i < 8; ++i) {
            const float a = (float)(16 * (i >> 2) + 4 * g + (i & 3)) * k1 * (1.f / 2048.f);
            t1c[i] = __builtin_amdgcn_cosf(a); t1s[i] = __builtin_amdgcn_sinf(a);
            const float b = (float)(16 * (i >> 2) + col) * (float)(16 * w + 4 * g + (i & 3)) * (1.f / 2048.f);
            t2c[i] = __builtin_amdgcn_cosf(b); t2s[i] = __builtin_amdgcn_sinf(b);
        }
    }
    char* xw = lds + (w >> 1) * 4096 + lane * 16 + (w & 1) * 8;
    {
        float ur[8], ui[8];
#pragma unroll
        for (int i = 0; i < 8; ++i) {
            const float rho = __builtin_amdgcn_rsqf(ss[i] * (1.f / 576.f) + 1e-5f);
            ur[i] = (j == 0 ? bhi(xo[i]) : blo(xo[i])) * rho;
            ui[i] = j == 0 ? 0.f : bhi(xo[i]) * rho;
        }
#pragma unroll
        for (int t = 0; t < 2; ++t) {
            *(LAS u32x2*)(xw + t * 2048) = u32x2{pk(ur[4 * t], ur[4 * t + 1]), pk(ur[4 * t + 2], ur[4 * t + 3])};
            *(LAS u32x2*)(xw + t * 2048 + 1024) = u32x2{pk(ui[4 * t], ui[4 * t + 1]), pk(ui[4 * t + 2], ui[4 * t + 3])};
        }
    }
    __syncthreads();
    const char* xr = lds + lane * 16;
    f32x4 yr[2] = {}, yi[2] = {};
#pragma unroll
    for (int t = 0; t < 2; ++t)
#pragma unroll
        for (int s = 0; s < 2; ++s) {
            const bf16x8 ar = asb(lds128(xr + s * 4096 + t * 2048)), ai = asb(lds128(xr + s * 4096 + t * 2048 + 1024));
            yr[t] = MF16(ar, asb(f64r[s]), yr[t]); yr[t] = MF16(ai, asb(neg8(f64i[s])), yr[t]);
            yi[t] = MF16(ar, asb(f64i[s]), yi[t]); yi[t] = MF16(ai, asb(f64r[s]), yi[t]);
        }
    u32x4 br, bi;
    {
        float vr[8], vi[8];
#pragma unroll
        for (int i = 0; i < 8; ++i) { const float a = yr[i >> 2][i & 3], b = yi[i >> 2][i & 3]; vr[i] = a * t1c[i] + b * t1s[i]; vi[i] = b * t1c[i] - a * t1s[i]; }
#pragma unroll
        for (int q = 0; q < 4; ++q) { br[q] = pk(vr[2 * q], vr[2 * q + 1]); bi[q] = pk(vi[2 * q], vi[2 * q + 1]); }
    }
#pragma unroll
    for (int t = 0; t < 2; ++t) {
        yr[t] = MF16(asb(f32r[t]), asb(br), f32x4{}); yr[t] = MF16(asb(neg8(f32i[t])), asb(bi), yr[t]);
        yi[t] = MF16(asb(f32i[t]), asb(br), f32x4{}); yi[t] = MF16(asb(f32r[t]), asb(bi), yi[t]);
    }
    {
        float vr[8], vi[8];
#pragma unroll
        for (int i = 0; i < 8; ++i) { const unsigned wv = i < 4 ? w0[i & 3] : w1[i & 3]; const float c = blo(wv) * (1.f / 2048.f), d = bhi(wv) * (1.f / 2048.f);
            const float a = yr[i >> 2][i & 3], b = yi[i >> 2][i & 3]; vr[i] = a * c - b * d; vi[i] = a * d + b * c; }
#pragma unroll
        for (int q = 0; q < 4; ++q) { br[q] = pk(vr[2 * q], vr[2 * q + 1]); bi[q] = pk(vi[2 * q], vi[2 * q + 1]); }
    }
#pragma unroll
    for (int t = 0; t < 2; ++t) {
        yr[t] = MF16(asb(br), asb(f32r[t]), f32x4{}); yr[t] = MF16(asb(bi), asb(f32i[t]), yr[t]);
        yi[t] = MF16(asb(bi), asb(f32r[t]), f32x4{}); yi[t] = MF16(asb(br), asb(neg8(f32i[t])), yi[t]);
    }
    {
        float vr[8], vi[8];
#pragma unroll
        for (int i = 0; i < 8; ++i) { const float a = yr[i >> 2][i & 3], b = yi[i >> 2][i & 3]; vr[i] = a * t2c[i] - b * t2s[i]; vi[i] = b * t2c[i] + a * t2s[i]; }
#pragma unroll
        for (int t = 0; t < 2; ++t) {
            *(LAS u32x2*)(xw + 8192 + t * 2048) = u32x2{pk(vr[4 * t], vr[4 * t + 1]), pk(vr[4 * t + 2], vr[4 * t + 3])};
            *(LAS u32x2*)(xw + 8192 + t * 2048 + 1024) = u32x2{pk(vi[4 * t], vi[4 * t + 1]), pk(vi[4 * t + 2], vi[4 * t + 3])};
        }
    }
    __syncthreads();
    yr[0] = f32x4{}; yr[1] = f32x4{}; yi[0] = f32x4{}; yi[1] = f32x4{};
#pragma unroll
    for (int t = 0; t < 2; ++t)
#pragma unroll
        for (int s = 0; s < 2; ++s) {
            const bf16x8 ur = asb(lds128(xr + 8192 + s * 4096 + t * 2048)), ui = asb(lds128(xr + 8192 + s * 4096 + t * 2048 + 1024));
            yr[t] = MF16(asb(f64r[s]), ur, yr[t]); yr[t] = MF16(asb(f64i[s]), ui, yr[t]);
            yi[t] = MF16(asb(f64r[s]), ui, yi[t]); yi[t] = MF16(asb(neg8(f64i[s])), ur, yi[t]);
        }
#pragma unroll
    for (int i = 0; i < 8; ++i) { const int n = 32 * (16 * w + 4 * g + (i & 3)) + 16 * (i >> 2) + col;
        const float a = yr[i >> 2][i & 3], b = yi[i >> 2][i & 3];
        const unsigned o = j == 0 ? pk(blo(xo[i]), bhi(xo[i]) + a) : pk(blo(xo[i]) + a, bhi(xo[i]) + b);
        *(unsigned*)(xout + cell0 + (size_t)n * 16) = o; }
}

constexpr float QSCALE = 0.23570226039551584f * 1.4426950408889634f;
template <bool LAST>
__global__ __launch_bounds__(512) void k_qkv(const char* __restrict__ xh, const char* __restrict__ wqh, char* __restrict__ kv, char* __restrict__ q1, char* __restrict__ q2,
                                             char* __restrict__ x3) {
    __shared__ __attribute__((aligned(16))) char lds[65536 + 24576 + 8 * 640];
    const int tid = threadIdx.x, lane = tid & 63, w = __builtin_amdgcn_readfirstlane(tid >> 6), r = lane & 31, hh = lane >> 5;
    const int bx = blockIdx.x, xcd = bx & 7, idx = bx >> 3, b = (xcd >> 1) * 2 + (idx & 1), h = (xcd & 1) * 16 + (idx >> 1);
    const int row = b * 256 + w * 32 + r;
    u32x4 xf[36];
#pragma unroll
    for (int s = 0; s < 36; ++s) xf[s] = *(const u32x4*)(xh + ((size_t)(2 * s + hh) * S + row) * 16);
    {
        const unsigned l0 = (unsigned)(uintptr_t)lds + w * 1024;
        const char* src = wqh + (size_t)h * HIMG + w * 1024 + lane * 16;
#pragma unroll
        for (int m = 0; m < 8; ++m) glds16(src + m * 8192, __builtin_amdgcn_readfirstlane(l0 + m * 8192));
        if (LAST) {
            const char* f2 = (const char*)&F2H + (size_t)h * 20736;
#pragma unroll
            for (int m = 0; m < 3; ++m) { const int o = (m * 8 + w) * 1024 + lane * 16; glds16(f2 + (o < 20736 ? o : 0), __builtin_amdgcn_readfirstlane(l0 + 65536 + m * 8192)); }
        }
    }
    WAIT_BAR(0);
    f32x16 aa = {}, ab = {}, ac = {};
    float q = 0.f;
    const char* pa = lds + lane * 16;
    const char* pb = lds + 1024 + hh * 352 + (r < 22 ? r : 21) * 16;
    const char* pc = lds + 65536 + hh * 288 + (r < 18 ? r : 17) * 16;
#pragma unroll
    for (int s = 0; s < 36; ++s) {
        aa = MF32(asb(lds128(pa + s * 1728)), asb(xf[s]), aa);
        ab = MF32(asb(xf[s]), asb(lds128(pb + s * 1728)), ab);
        if (LAST) ac = MF32(asb(lds128(pc + s * 576)), asb(xf[s]), ac);
#pragma unroll
        for (int i = 0; i < 4; ++i) { unsigned v = xf[s][i]; if (s == 0 && i == 0 && hh == 0) v &= 0xFFFF0000u;
            q = sq2(v, q); }
    }
    q += __shfl_xor(q, 32);
    const float rho = __builtin_amdgcn_rsqf(q * (1.f / 576.f) + 1e-5f);
    float* sc = (float*)(lds + 65536 + 24576 + w * 640);
    if (hh == 0) *(LAS float*)(sc + r) = rho;
    if (r >= 18 && r < 22) {
#pragma unroll
        for (int i = 0; i < 16; ++i) *(LAS float*)(sc + 32 + ((i & 3) + 8 * (i >> 2) + 4 * hh) * 4 + (r - 18)) = ab[i];
    }
    const float rq = rho * QSCALE;
    char* kvc = kv + ((size_t)h * 8 + b) * KVCH;
    *(u32x4*)(q1 + (((size_t)h * 64 + b * 8 + w) * 64 + lane) * 16) = u32x4{pk(aa[0] * rq, aa[1] * rq), pk(aa[2] * rq, aa[3] * rq), pk(aa[4] * rq, aa[5] * rq), pk(aa[6] * rq, aa[7] * rq)};
    *(u32x4*)(kvc + w * 1024 + lane * 16) = u32x4{pk(aa[8] * rho, aa[9] * rho), pk(aa[10] * rho, aa[11] * rho), pk(aa[12] * rho, aa[13] * rho), pk(aa[14] * rho, aa[15] * rho)};
    if (LAST) {
        *(u32x4*)(x3 + ((size_t)(2 * h + hh) * S + row) * 16) = u32x4{pk(ac[0], ac[1]), pk(ac[2], ac[3]), pk(ac[4], ac[5]), pk(ac[6], ac[7])};
        if (hh == 0) *(unsigned*)(x3 + ((size_t)(64 + (h >> 2)) * S + row) * 16 + (h & 3) * 4) = pk(ac[8], ac[9]);
    }
    asm volatile("s_waitcnt lgkmcnt(0)" ::: "memory");
    const f32x4 lo = *(const LAS f32x4*)(sc + 32 + r * 4);
    if (hh == 0) *(unsigned*)(q2 + ((size_t)h * S + row) * 4) = pk(lo[0] * rq, lo[1] * rq);
    else *(unsigned*)(kvc + 8192 + (w * 32 + r) * 4) = pk(lo[2] * rho, lo[3] * rho);
    f32x4 rr[4];
#pragma unroll
    for (int i = 0; i < 4; ++i) rr[i] = *(const LAS f32x4*)(sc + 8 * i + 4 * hh);
    if (r < 18) {
#pragma unroll
        for (int s = 0; s < 2; ++s)
            *(u32x4*)(kvc + 9216 + w * 1152 + s * 576 + hh * 288 + r * 16) =
                u32x4{pk(ab[8 * s] * rr[2 * s][0], ab[8 * s + 1] * rr[2 * s][1]), pk(ab[8 * s + 2] * rr[2 * s][2], ab[8 * s + 3] * rr[2 * s][3]),
                      pk(ab[8 * s + 4] * rr[2 * s + 1][0], ab[8 * s + 5] * rr[2 * s + 1][1]), pk(ab[8 * s + 6] * rr[2 * s + 1][2], ab[8 * s + 7] * rr[2 * s + 1][3])};
    }
}

#define EX(x) __builtin_amdgcn_exp2f(x)
__device__ __forceinline__ float xhalf_max(float m) {
    auto rr = __builtin_amdgcn_permlane32_swap(__float_as_uint(m), __float_as_uint(m), false, false);
    return fmaxf(__uint_as_float(rr[0]), __uint_as_float(rr[1]));
}
__device__ __forceinline__ float max16(const f32x16& s) {
    float a = fmaxf(fmaxf(s[0], s[1]), s[2]), b = fmaxf(fmaxf(s[3], s[4]), s[5]);
    a = fmaxf(fmaxf(a, s[6]), s[7]); b = fmaxf(fmaxf(b, s[8]), s[9]); a = fmaxf(fmaxf(a, s[10]), s[11]); b = fmaxf(fmaxf(b, s[12]), s[13]);
    a = fmaxf(fmaxf(a, s[14]), s[15]);
    return xhalf_max(fmaxf(a, b));
}
constexpr int ATT_CONST = 8 * (int)KVCH;
__global__ __launch_bounds__(512) void k_att(const char* __restrict__ kv, const char* __restrict__ q1, const char* __restrict__ q2, char* __restrict__ o) {
    __shared__ __attribute__((aligned(256))) char lds[ATT_CONST + 9216 + 256];
    const int tid = threadIdx.x, lane = tid & 63, w = __builtin_amdgcn_readfirstlane(tid >> 6), r = lane & 31, hh = lane >> 5;
    const int bx = blockIdx.x, h = (bx & 7) * 4 + (bx >> 6), b = (bx >> 3) & 7, row = b * 256 + w * 32 + r;
    const u32x4 qf = *(const u32x4*)(q1 + (((size_t)h * 64 + b * 8 + w) * 64 + lane) * 16);
    const unsigned q67 = *(const unsigned*)(q2 + ((size_t)h * S + row) * 4);
    {
        const unsigned l0 = (unsigned)(uintptr_t)lds + w * 1024;
        const char* src = kv + (size_t)h * 8 * KVCH + w * 1024 + lane * 16;
#pragma unroll
        for (int m = 0; m < 18; ++m) glds16(src + m * 8192, __builtin_amdgcn_readfirstlane(l0 + m * 8192));
    }
    if (tid < 128) { const unsigned v = (tid & 1) ? 0u : 0x3F803F80u; *(LAS u32x4*)(lds + ATT_CONST + (tid >> 2) * 288 + (2 + (tid & 3)) * 16) = u32x4{v, v, v, v}; }
    const bool isv = r < 18;
    const int cslot = ((r >= 20 && r < 28) ? 4 : 2) + ((r == 18 || r == 20) ? 0 : 1);
    const char* vp = lds + (isv ? 9216 + hh * 288 + r * 16 : ATT_CONST + hh * 288 + cslot * 16);
    const int vstep = isv ? (int)KVCH : 0;
    const char* kp = lds + lane * 16;
    const char* k2p = lds + 8192 + lane * 4;
    asm volatile("" :: "v"(qf), "v"(q67));
    WAIT_BAR(15);
    u32x4 k2 = {*(const LAS unsigned*)k2p, 0x00003F80u, 0u, 0u};
    u32x4 qa = {hh ? 0u : q67, 0u, 0u, 0u}, qb = {hh ? q67 : 0u, 0u, 0u, 0u};
    u32x4 k1 = lds128(kp);
    f32x16 sA, sB, acc = {};
    sA = MF32(asb(k1), asb(qf), f32x16{}); sA = MF32(asb(k2), asb(qa), sA);
    {
        const unsigned nm = pk(-max16(sA), 0.f) & 0xFFFFu;
        qa[1] = hh ? 0u : nm; qb[1] = hh ? nm : 0u;
    }
    sA = MF32(asb(k1), asb(qf), f32x16{}); sA = MF32(asb(k2), asb(qa), sA);
    u32x4 p0, p1 = {}, v0, v1 = {};
    k1 = lds128(kp + 1024);
#define STEP(SC, SN, T, QN) do { \
        float e0, e1, e2, e3; \
        SN = MF32(asb(k1), asb(qf), f32x16{}); v0 = lds128(vp + (T) * 1152); SB(); \
        e0 = EX(SC[0]); e1 = EX(SC[1]); e2 = EX(SC[2]); e3 = EX(SC[3]); p0[0] = pk(e0, e1); p0[1] = pk(e2, e3); SB(); \
        acc = MF32(asb(v1), asb(p1), acc); k1 = lds128(kp + ((T) + 2 < 8 ? ((T) + 2) * 1024 : (int)KVCH + ((T) - 6) * 1024)); SB(); \
        e0 = EX(SC[4]); e1 = EX(SC[5]); e2 = EX(SC[6]); e3 = EX(SC[7]); p0[2] = pk(e0, e1); p0[3] = pk(e2, e3); SB(); \
        SN = MF32(asb(k2), asb(QN), SN); v1 = lds128(vp + (T) * 1152 + 576); SB(); \
        e0 = EX(SC[8]); e1 = EX(SC[9]); e2 = EX(SC[10]); e3 = EX(SC[11]); p1[0] = pk(e0, e1); p1[1] = pk(e2, e3); SB(); \
        acc = MF32(asb(v0), asb(p0), acc); if (!((T) & 1)) k2[0] = *(const LAS unsigned*)(k2p + ((T) + 2 < 8 ? ((T) + 2) * 128 : (int)KVCH)); SB(); \
        e0 = EX(SC[12]); e1 = EX(SC[13]); e2 = EX(SC[14]); e3 = EX(SC[15]); p1[2] = pk(e0, e1); p1[3] = pk(e2, e3); SB(); \
    } while (0)
    for (int c = 0; c < 8; ++c) {
        STEP(sA, sB, 0, qb); STEP(sB, sA, 1, qa); STEP(sA, sB, 2, qb); STEP(sB, sA, 3, qa); STEP(sA, sB, 4, qb); STEP(sB, sA, 5, qa);
        if (c == 0) WAIT_BAR(0);
        STEP(sA, sB, 6, qb); STEP(sB, sA, 7, qa);
        kp += KVCH; k2p += KVCH; vp += vstep;
    }
#undef STEP
    acc = MF32(asb(v1), asb(p1), acc);
    float l = hh ? acc[8] : acc[10];
    bool ok = l > 1e-12f && l < 1e30f;
#pragma unroll
    for (int i = 0; i < 10; ++i) ok = ok && (fabsf(acc[i]) < 3e38f);
    if (__builtin_expect(!__all(ok), 0)) {
        qa[1] = 0u; qb[1] = 0u; acc = f32x16{};
        float m = -3.0e38f;
        kp = lds + lane * 16; k2p = lds + 8192 + lane * 4; vp -= 8 * vstep;
        for (int t = 0; t < 64; ++t) {
            const int co = (t >> 3) * (int)KVCH, tt = t & 7;
            k2[0] = *(const LAS unsigned*)(k2p + co + (tt >> 1) * 256);
            sA = MF32(asb(lds128(kp + co + tt * 1024)), asb(qf), f32x16{});
            sA = MF32(asb(k2), asb((tt & 1) ? qb : qa), sA);
            const float mn = fmaxf(m, max16(sA)), al = EX(m - mn);
            m = mn;
#pragma unroll
            for (int i = 0; i < 16; ++i) acc[i] *= al;
#pragma unroll
            for (int i = 0; i < 4; ++i) { p0[i] = pk(EX(sA[2 * i] - mn), EX(sA[2 * i + 1] - mn)); p1[i] = pk(EX(sA[8 + 2 * i] - mn), EX(sA[9 + 2 * i] - mn)); }
            const char* vq = vp + (t >> 3) * vstep + tt * 1152;
            acc = MF32(asb(lds128(vq)), asb(p0), acc); acc = MF32(asb(lds128(vq + 576)), asb(p1), acc);
        }
        l = hh ? acc[8] : acc[10];
    }
    const float il = 1.f / l;
    *(u32x4*)(o + ((size_t)(2 * h + hh) * S + row) * 16) = u32x4{pk(acc[0] * il, acc[1] * il), pk(acc[2] * il, acc[3] * il), pk(acc[4] * il, acc[5] * il), pk(acc[6] * il, acc[7] * il)};
    if (hh == 0) *(unsigned*)(o + ((size_t)(64 + (h >> 2)) * S + row) * 16 + (h & 3) * 4) = pk(acc[8] * il, acc[9] * il);
}

template <bool LAST>
__global__ __launch_bounds__(256) void k_oproj(const char* __restrict__ o, const char* __restrict__ wimg, const char* __restrict__ res, char* __restrict__ out, float* __restrict__ ssp) {
    __shared__ __attribute__((aligned(16))) char lds[86016];
    const int tid = threadIdx.x, lane = tid & 63, wid = __builtin_amdgcn_readfirstlane(tid >> 6), bx = blockIdx.x;
    const int idx = bx >> 3, rb = (bx & 7) * 4 + (idx >> 3), a = idx & 7, p = lane & 15, g = lane >> 4, r = rb * 64 + wid * 16 + p;
    u32x4 rf[18];
    const u32x4 e0 = *(const u32x4*)(res + ((size_t)(9 * a + g) * S + r) * 16), e1 = *(const u32x4*)(res + ((size_t)(9 * a + 4 + g) * S + r) * 16);
    const u32x2 e2 = *(const u32x2*)(res + ((size_t)(9 * a + 8) * S + r) * 16 + (g & 1) * 8);
#pragma unroll
    for (int s = 0; s < 18; ++s) rf[s] = *(const u32x4*)(o + ((size_t)(4 * s + g) * S + r) * 16);
    gemm72_issue(wimg + a * GIMG, lds, wid, lane);
    f32x4 acc[5] = {{blo(e0[0]), bhi(e0[0]), blo(e0[1]), bhi(e0[1])}, {blo(e0[2]), bhi(e0[2]), blo(e0[3]), bhi(e0[3])},
                    {blo(e1[0]), bhi(e1[0]), blo(e1[1]), bhi(e1[1])}, {blo(e1[2]), bhi(e1[2]), blo(e1[3]), bhi(e1[3])},
                    {blo(e2[0]), bhi(e2[0]), blo(e2[1]), bhi(e2[1])}};
    gemm72_run(lds, lane, rf, acc);
    if (!LAST) store_xh(acc, out, ssp, a, r, g);
    else {
        char* d = out + ((size_t)r * D + 72 * a) * 2;
#pragma unroll
        for (int u = 0; u < 2; ++u)
            *(u32x4*)(d + (32 * u + 8 * g) * 2) = u32x4{pk(acc[2 * u][0], acc[2 * u][1]), pk(acc[2 * u][2], acc[2 * u][3]), pk(acc[2 * u + 1][0], acc[2 * u + 1][1]), pk(acc[2 * u + 1][2], acc[2 * u + 1][3])};
        if (g < 2) *(u32x2*)(d + 128 + g * 8) = u32x2{pk(acc[4][0], acc[4][1]), pk(acc[4][2], acc[4][3])};
    }
}

extern "C" void kernel_launch(void* const* d_in, const int* in_sizes, int n_in, void* d_out, int out_size, void* d_ws, size_t ws_size, hipStream_t stream) {
    const u16* x = (const u16*)d_in[0]; const u16* wr = (const u16*)d_in[1]; const u16* wi = (const u16*)d_in[2];
    const u16* wqkv = (const u16*)d_in[4]; const u16* wo = (const u16*)d_in[5];
    char* ws = (char*)d_ws;
    k_in<<<256, 512, 0, stream>>>(x, wr, wi, wqkv, wo, ws);
    k_wprep<<<288, 256, 0, stream>>>(ws);
    k_fft<<<288, 256, 0, stream>>>(ws + O_XH0, ws + O_XH1, (const float*)(ws + O_SS0), ws + O_WT);
    k_qkv<false><<<256, 512, 0, stream>>>(ws + O_XH1, ws + O_WQH, ws + O_KVA, ws + O_Q1A, ws + O_Q2A, ws + O_X3);
    k_att<<<256, 512, 0, stream>>>(ws + O_KVA, ws + O_Q1A, ws + O_Q2A, ws + O_OA);
    k_oproj<false><<<256, 256, 0, stream>>>(ws + O_OA, ws + O_WOH, ws + O_XH1, ws + O_XH2, (float*)(ws + O_SS2));
    k_fft<<<288, 256, 0, stream>>>(ws + O_XH2, ws + O_XH3, (const float*)(ws + O_SS2), ws + O_WT);
    k_qkv<true><<<256, 512, 0, stream>>>(ws + O_XH3, ws + O_WQH, ws + O_KVB, ws + O_Q1B, ws + O_Q2B, ws + O_X3);
    k_att<<<256, 512, 0, stream>>>(ws + O_KVB, ws + O_Q1B, ws + O_Q2B, ws + O_OB);
    k_oproj<true><<<256, 256, 0, stream>>>(ws + O_OB, ws + O_WO2, ws + O_X3, (char*)d_out, (float*)(ws + O_SS2));
}
```

```cpp
#include <hip/hip_runtime.h>
#include <hip/hip_bf16.h>
#include <stdint.h>
#include <utility>

typedef __attribute__((ext_vector_type(8))) __bf16 bf16x8;
typedef __attribute__((ext_vector_type(2))) __bf16 bf16x2;
typedef __attribute__((ext_vector_type(2))) float f32x2;
typedef __attribute__((ext_vector_type(4))) float f32x4;
typedef __attribute__((ext_vector_type(16))) float f32x16;
typedef __attribute__((ext_vector_type(8))) float f32x8;
typedef __attribute__((ext_vector_type(16))) __bf16 bf16x16;
typedef __attribute__((ext_vector_type(4))) unsigned u32x4;
typedef __attribute__((ext_vector_type(2))) unsigned u32x2;
typedef __attribute__((ext_vector_type(3))) unsigned u32x3;
typedef unsigned short u16;
typedef __attribute__((address_space(1))) unsigned gu32;
typedef __amdgpu_buffer_rsrc_t rsrc_t;
#define LAS __attribute__((address_space(3)))

constexpr int S = 2048, D = 576, NH = 32;

namespace tbl {
constexpr double PI = 3.14159265358979323846264338327950288;
constexpr double ccos(int m) {
    if (m > 288) m = 576 - m;
    double sg = 1.0;
    if (m > 144) { m = 288 - m; sg = -1.0; }
    if (m == 144) return 0.0;
    const double x = 2.0 * PI * m / 576.0, x2 = x * x;
    double term = 1.0, sum = 1.0;
    for (int i = 1; i < 16; ++i) { term *= -x2 / ((2 * i - 1) * (2 * i)); sum += term; }
    return sg * sum;
}
constexpr u16 tobf(double v) {
    const unsigned u = __builtin_bit_cast(unsigned, (float)v);
    return (u16)((u + 0x7FFFu + ((u >> 16) & 1u)) >> 16);
}
struct Base { u16 c[576], u[576]; };
constexpr double SC = 0.05892556509887896;
constexpr Base mkbase() { Base b{}; for (int m = 0; m < 576; ++m) { b.c[m] = tobf(SC * ccos(m)); b.u[m] = tobf(ccos(m)); } return b; }
constexpr Base base = mkbase();
constexpr u16 DCV = tobf(1.0 / 24.0), NDCV = tobf(-1.0 / 24.0);
constexpr int freq(int ch) {
    const int kb = ch >> 2, cc = ch & 3;
    const int w = kb < 64 ? kb >> 4 : (kb - 64) >> 1, g = kb < 64 ? kb & 3 : 2 * (kb & 1) + (cc >> 1), tau = kb < 64 ? 2 * ((kb >> 2) & 3) + (cc >> 1) : 8;
    const int sg = 3 * w + tau / 3, d = 8 * (tau % 3) + 2 * g + (cc & 1);
    if (sg) return d < 12 ? sg + 24 * d : 576 - sg - 24 * d;
    return d < 12 ? 24 * d : 12 + 24 * (d - 12);
}
constexpr u16 Fval(int n, int c) {
    if (c < 2) return (c == 0 || !(n & 1)) ? DCV : NDCV;
    return base.c[(freq(c >> 1) * n + ((c & 1) ? 144 : 0)) % 576];
}
struct Chunk { u16 v[4608]; };
template <int I> constexpr Chunk mkF2() {
    Chunk c{}; int o = 0;
    for (int q = 0; q < 16; ++q) { const int hs = I * 16 + q, h = hs / 36, s = hs % 36;
        for (int hh = 0; hh < 2; ++hh) for (int p = 0; p < 18; ++p) {
            const int n = p < 16 ? 16 * h + 8 * ((p >> 2) & 1) + (p & 3) + 4 * (p >> 3) : 512 + 2 * h + (p - 16);
            for (int e = 0; e < 8; ++e) c.v[o++] = Fval(n, 16 * s + 8 * hh + e); } }
    return c;
}
template <int I> constexpr Chunk mkT1() {
    Chunk c{}; int o = 0;
    for (int b = 6 * I; b < 6 * I + 6; ++b) for (int t = 0; t < 2; ++t) for (int l = 0; l < 64; ++l) for (int e = 0; e < 6; ++e) {
        const int ro = l & 15, a = 4 * e + (l >> 4), n = 24 * a + b;
        c.v[o++] = ro >= 12 ? (u16)0 : (t && !ro) ? base.u[(a & 1) * 288] : base.u[(ro * n + 144 * t) % 576]; }
    return c;
}
constexpr Chunk mkT2() {
    Chunk c{}; int o = 0;
    for (int sp = 0; sp < 2; ++sp) for (int T = 0; T < 3; ++T) for (int t = 0; t < 2; ++t) for (int l = 0; l < 64; ++l) for (int e = 0; e < 6; ++e) {
        const int m = l & 15, d = 8 * T + (m >> 1), po = m & 1, b = 6 * (l >> 4) + 3 * t + (e >> 1), pi = e & 1;
        u16 v = 0;
        if (!sp) v = base.c[(24 * b * d + (po ? (pi ? 0 : 144) + (d >= 12 ? 288 : 0) : (pi ? 432 : 0))) % 576];
        else if (d == 0) v = pi ? (u16)0 : (po && (b & 1)) ? NDCV : DCV;
        else if (d < 12) v = pi ? (u16)0 : base.c[(24 * b * d + 144 * po) % 576];
        else v = pi ? base.c[((12 + 24 * (d - 12)) * b + 144 * po) % 576] : (u16)0;
        c.v[o++] = v; }
    return c;
}
template <int I> constexpr Chunk f2c = mkF2<I>();
struct Table { Chunk c[72]; };
struct Small { Chunk c[5]; };
template <int... I> constexpr Table mkF2T(std::integer_sequence<int, I...>) { return Table{{f2c<I>...}}; }
}
__device__ __attribute__((aligned(256))) const tbl::Table F2H = tbl::mkF2T(std::make_integer_sequence<int, 72>{});
__device__ __attribute__((aligned(256))) const tbl::Small TFF = {{tbl::mkT1<0>(), tbl::mkT1<1>(), tbl::mkT1<2>(), tbl::mkT1<3>(), tbl::mkT2()}};

constexpr size_t CELLS = (size_t)72 * S * 16;
constexpr size_t GIMG = 82944;
constexpr size_t HIMG = 62208;
constexpr size_t KVCH = 18432;
constexpr size_t al(size_t x) { return (x + 4095) & ~(size_t)4095; }
constexpr size_t O_XH0 = 0, O_XH1 = O_XH0 + CELLS, O_XH2 = O_XH1 + CELLS, O_XH3 = O_XH2 + CELLS;
constexpr size_t O_OA = O_XH3 + CELLS, O_OB = O_OA + CELLS, O_X3 = O_OB + CELLS;
constexpr size_t O_WT = O_X3 + CELLS;
constexpr size_t O_WO2 = O_WT + (size_t)256 * 288 * 32;
constexpr size_t O_WOH = O_WO2 + al(8 * GIMG + 4096);
constexpr size_t O_WQH = O_WOH + al(8 * GIMG + 4096);
constexpr size_t O_KVA = O_WQH + al(32 * HIMG + 4096), O_KVB = O_KVA + 256 * KVCH;
constexpr size_t O_SS0 = O_KVB + 256 * KVCH, O_SS2 = O_SS0 + (size_t)S * 16;
constexpr size_t O_FLG = O_SS2 + (size_t)S * 16;
constexpr size_t O_DBG = O_FLG + 65536;

__device__ __forceinline__ unsigned pk(float a, float b) { f32x2 t = {a, b}; return __builtin_bit_cast(unsigned, __builtin_convertvector(t, bf16x2)); }
__device__ __forceinline__ float blo(unsigned u) { return __uint_as_float(u << 16); }
__device__ __forceinline__ float bhi(unsigned u) { return __uint_as_float(u & 0xFFFF0000u); }
__device__ __forceinline__ float sq2(unsigned v, float q) { const bf16x2 t = __builtin_bit_cast(bf16x2, v); return __builtin_amdgcn_fdot2_f32_bf16(t, t, q, false); }
__device__ __forceinline__ float sum2(unsigned v, float q) { return __builtin_amdgcn_fdot2_f32_bf16(__builtin_bit_cast(bf16x2, v), __builtin_bit_cast(bf16x2, 0x3F803F80u), q, false); }
__device__ __forceinline__ rsrc_t mkrsrc(const void* p, int bytes) { return __builtin_amdgcn_make_buffer_rsrc((void*)p, (short)0, bytes, 0x00020000); }
__device__ __forceinline__ bf16x8 asb(u32x4 v) { return __builtin_bit_cast(bf16x8, v); }
__device__ __forceinline__ void glds16(const void* g, unsigned lds_base) {
    unsigned sv;
    asm volatile("s_mov_b32 %0, m0\n\ts_mov_b32 m0, %2\n\ts_nop 0\n\tglobal_load_lds_dwordx4 %1, off\n\ts_mov_b32 m0, %0" : "=&s"(sv) : "v"(g), "s"(lds_base) : "memory");
}
#define WAIT_BAR(N) asm volatile("s_waitcnt vmcnt(" #N ") lgkmcnt(0)\n\ts_barrier" ::: "memory")
#define MF16(a, b, c) __builtin_amdgcn_mfma_f32_16x16x32_bf16(a, b, c, 0, 0, 0)
#define MF32(a, b, c) __builtin_amdgcn_mfma_f32_32x32x16_bf16(a, b, c, 0, 0, 0)
#define SB() __builtin_amdgcn_sched_barrier(0)
__device__ __forceinline__ u32x4 lds128(const char* p) { return *(const LAS u32x4*)(p); }
__device__ __forceinline__ unsigned gimg_off(int kb, int c) {
    const int a = c / 72, x = c - 72 * a, s = kb >> 2, g = kb & 3;
    unsigned o;
    if (x < 64) { const int T = 2 * (x >> 5) + ((x >> 2) & 1), p = 4 * ((x >> 3) & 3) + (x & 3); o = T * 1024 + g * 256 + p * 16; }
    else o = 4096 + g * 128 + (x - 64) * 16;
    return a * (unsigned)GIMG + s * 4608 + o;
}
__device__ __forceinline__ int ofeat(int kb, int e) {
    return kb < 64 ? 18 * (kb >> 1) + (e & 3) + 8 * (e >> 2) + 4 * (kb & 1) : 18 * (4 * (kb - 64) + (e >> 1)) + 16 + (e & 1);
}

__device__ __forceinline__ void gemm72_issue(const char* cimg, char* lds, int wid, int lane) {
    const unsigned l0 = (unsigned)(uintptr_t)lds + wid * 1024;
    const char* src = cimg + wid * 1024 + lane * 16;
#pragma unroll
    for (int m = 0; m < 21; ++m) glds16(src + m * 4096, __builtin_amdgcn_readfirstlane(l0 + m * 4096));
}
__device__ __forceinline__ void gemm72_steps(const char* lds, int lane, const u32x4 (&rf)[18], f32x4 (&acc)[5], int s0) {
    const char* p = lds + lane * 16;
    const char* p4 = lds + 4096 + (lane >> 4) * 128 + (lane & 7) * 16;
#pragma unroll
    for (int ss = 0; ss < 6; ++ss) {
        const int s = s0 + ss;
#pragma unroll
        for (int T = 0; T < 4; ++T) acc[T] = MF16(asb(lds128(p + s * 4608 + T * 1024)), asb(rf[s]), acc[T]);
        acc[4] = MF16(asb(lds128(p4 + s * 4608)), asb(rf[s]), acc[4]);
    }
}
__device__ __forceinline__ void gemm72_run(const char* lds, int lane, const u32x4 (&rf)[18], f32x4 (&acc)[5]) {
    WAIT_BAR(14); gemm72_steps(lds, lane, rf, acc, 0);
    WAIT_BAR(7);  gemm72_steps(lds, lane, rf, acc, 6);
    WAIT_BAR(0);  gemm72_steps(lds, lane, rf, acc, 12);
}

__device__ __forceinline__ void store_xh(f32x4 (&acc)[5], char* xh, char* ssp, int a, int r, int g) {
    float q = 0.f;
#pragma unroll
    for (int u = 0; u < 2; ++u) {
        u32x4 o = {pk(acc[2 * u][0], acc[2 * u][1]), pk(acc[2 * u][2], acc[2 * u][3]), pk(acc[2 * u + 1][0], acc[2 * u + 1][1]), pk(acc[2 * u + 1][2], acc[2 * u + 1][3])};
        *(u32x4*)(xh + ((size_t)(9 * a + 4 * u + g) * S + r) * 16) = o;
        if (u == 0 && a == 0 && g == 0) o[0] &= 0xFFFF0000u;
#pragma unroll
        for (int i = 0; i < 4; ++i) q = sq2(o[i], q);
    }
    if (g < 2) {
        u32x2 o = {pk(acc[4][0], acc[4][1]), pk(acc[4][2], acc[4][3])};
        *(u32x2*)(xh + ((size_t)(9 * a + 8) * S + r) * 16 + g * 8) = o;
        q = sq2(o[0], q);
        q = sq2(o[1], q);
    }
    q += __shfl_xor(q, 16); q += __shfl_xor(q, 32);
    if (g == 0) *(u16*)(ssp + r * 16 + a * 2) = (u16)pk(q, 0.f);
}

__device__ __forceinline__ unsigned himg_off(int kb, int j) {
    const int which = j / 576, rem = j - which * 576, h = rem / 18, d = rem - h * 18, s = kb >> 1, hh = kb & 1;
    unsigned o;
    if (which < 2 && d < 16) o = hh * 512 + (16 * which + (d & 3) + 8 * ((d >> 2) & 1) + 4 * (d >> 3)) * 16;
    else o = 1024 + hh * 352 + (which == 2 ? d : 2 + 2 * which + d) * 16;
    return h * (unsigned)HIMG + s * 1728 + o;
}

constexpr int B_WQ = 128, B_WO = B_WQ + 112, B_WT = B_WO + 40, B_W2 = B_WT + 144, B_END = B_W2 + 81;
__global__ __launch_bounds__(256) void k_in(const u16* __restrict__ x, const u16* __restrict__ wr, const u16* __restrict__ wi,
                                            const u16* __restrict__ wqkv, const u16* __restrict__ wo, char* __restrict__ ws) {
    __shared__ __attribute__((aligned(16))) char lds[24576];
    const int tid = threadIdx.x, lane = tid & 63, w = __builtin_amdgcn_readfirstlane(tid >> 6), bx = blockIdx.x, v = lane & 15, g = lane >> 4;
    if (bx < B_WT) {
        const int mode = bx < B_WQ ? 0 : bx < B_WO ? 1 : 2;
        const int vb = mode == 0 ? bx : mode == 1 ? (bx & 7) * 14 + ((bx - B_WQ) >> 3) : bx - B_WO;
        if (vb >= (mode == 1 ? 108 : mode == 2 ? 36 : 128)) return;
        const int vec = vb * 16 + v;
        u32x4 df[6];
        if (mode != 1) {
            const rsrc_t rs = mkrsrc(mode ? wo : x, mode ? D * D * 2 : S * D * 2);
            const int off = (mode ? ofeat(vec >> 3, vec & 7) : vec) * 1152 + 48 * g + 12 * w;
            u32x3 raw[6];
#pragma unroll
            for (int e = 0; e < 6; ++e) raw[e] = __builtin_amdgcn_raw_buffer_load_b96(rs, off + 192 * e, 0, 0);
#pragma unroll
            for (int bp = 0; bp < 3; ++bp)
#pragma unroll
                for (int k = 0; k < 3; ++k) {
                    df[2 * bp][k] = __builtin_amdgcn_perm(raw[2 * k + 1][bp], raw[2 * k][bp], 0x05040100u);
                    df[2 * bp + 1][k] = __builtin_amdgcn_perm(raw[2 * k + 1][bp], raw[2 * k][bp], 0x07060302u);
                }
        } else {
            const u16* src = wqkv + (size_t)(24 * g + 6 * w) * 1728 + vec;
#pragma unroll
            for (int bl = 0; bl < 6; ++bl)
#pragma unroll
                for (int k = 0; k < 3; ++k) df[bl][k] = src[(192 * k + bl) * 1728] | ((unsigned)src[(192 * k + 96 + bl) * 1728] << 16);
        }
        const rsrc_t rt = mkrsrc(&TFF, sizeof(TFF));
        u32x4 t1[12], t2[6], t0[6];
#pragma unroll
        for (int i = 0; i < 12; ++i) { const u32x3 t = __builtin_amdgcn_raw_buffer_load_b96(rt, lane * 12 + i * 768, w * 9216, 0); t1[i] = u32x4{t[0], t[1], t[2], 0u}; }
#pragma unroll
        for (int i = 0; i < 6; ++i) {
            const u32x3 t = __builtin_amdgcn_raw_buffer_load_b96(rt, lane * 12 + i * 768, 36864, 0); t2[i] = u32x4{t[0], t[1], t[2], 0u};
            const u32x3 z = __builtin_amdgcn_raw_buffer_load_b96(rt, lane * 12 + i * 768, w ? 36864 : 41472, 0); t0[i] = u32x4{z[0], z[1], z[2], 0u};
        }
        unsigned y[4][6];
#pragma unroll
        for (int bl = 0; bl < 6; ++bl) {
            df[bl][3] = 0u;
            const f32x4 re = MF16(asb(t1[2 * bl]), asb(df[bl]), f32x4{}), im = MF16(asb(t1[2 * bl + 1]), asb(df[bl]), f32x4{});
#pragma unroll
            for (int r = 0; r < 4; ++r) y[r][bl] = pk(re[r], im[r]);
        }
        if (g < 3) {
#pragma unroll
            for (int r = 0; r < 4; ++r)
#pragma unroll
                for (int t = 0; t < 2; ++t) *(LAS u32x4*)(lds + (4 * g + r) * 2048 + t * 1024 + w * 256 + v * 16) = u32x4{y[r][3 * t], y[r][3 * t + 1], y[r][3 * t + 2], 0u};
        }
        __syncthreads();
        f32x4 acc[9];
#pragma unroll
        for (int sl = 0; sl < 3; ++sl) {
            const u32x4 y0 = lds128(lds + (3 * w + sl) * 2048 + lane * 16), y1 = lds128(lds + (3 * w + sl) * 2048 + 1024 + lane * 16);
#pragma unroll
            for (int T = 0; T < 3; ++T) {
                const u32x4 m0 = sl ? t2[2 * T] : t0[2 * T], m1 = sl ? t2[2 * T + 1] : t0[2 * T + 1];
                if (mode != 2) acc[3 * sl + T] = MF16(asb(m1), asb(y1), MF16(asb(m0), asb(y0), f32x4{}));
                else acc[3 * sl + T] = MF16(asb(y1), asb(m1), MF16(asb(y0), asb(m0), f32x4{}));
            }
        }
        u32x2 o[9];
#pragma unroll
        for (int i = 0; i < 9; ++i) o[i] = u32x2{pk(acc[i][0], acc[i][1]), pk(acc[i][2], acc[i][3])};
        if (mode == 0) {
            char* xh = ws + O_XH0 + (size_t)vec * 16;
#pragma unroll
            for (int u = 0; u < 4; ++u) *(u32x4*)(xh + (size_t)(16 * w + 4 * u + g) * (S * 16)) = u32x4{o[2 * u][0], o[2 * u][1], o[2 * u + 1][0], o[2 * u + 1][1]};
            *(u32x2*)(xh + (size_t)(64 + 2 * w + (g >> 1)) * (S * 16) + (g & 1) * 8) = o[8];
            if (w == 0 && g == 0) o[0][0] &= 0xFFFF0000u;
            float q = 0.f;
#pragma unroll
            for (int i = 0; i < 9; ++i) { q = sq2(o[i][0], q); q = sq2(o[i][1], q); }
            q += __shfl_xor(q, 16); q += __shfl_xor(q, 32);
            if (g == 0) *(unsigned*)(ws + O_SS0 + vec * 16 + w * 4) = pk(q, 0.f);
        } else if (mode == 1) {
            if (w == 0 && g == 0) o[0][0] &= 0xFFFF0000u;
            char* d = ws + O_WQH + himg_off(16 * w + g, vec);
#pragma unroll
            for (int u = 0; u < 4; ++u) *(u32x4*)(d + u * 3456) = u32x4{o[2 * u][0], o[2 * u][1], o[2 * u + 1][0], o[2 * u + 1][1]};
            *(u32x2*)(ws + O_WQH + himg_off(64 + 2 * w + (g >> 1), vec) + (g & 1) * 8) = o[8];
        } else {
#pragma unroll
            for (int i = 0; i < 9; ++i) {
                const int c = i < 8 ? 8 * (16 * w + 4 * (i >> 1) + (v >> 2)) + 4 * (i & 1) + (v & 3) : 512 + 16 * w + v;
                *(u32x2*)(ws + O_WOH + gimg_off(2 * vb + (g >> 1), c) + (g & 1) * 8) = o[i];
            }
        }
    } else if (bx < B_W2) {
        unsigned o[2][8]; char* d[2];
#pragma unroll
        for (int u = 0; u < 2; ++u) {
            const int L = (bx - B_WT) * 128 + 64 * u + lane, qd = L / 288, jj = L - qd * 288, t = 4 * qd + w;
            const int k0 = (16 * (t >> 6) + (t & 15) + 256 * ((t >> 4) & 3)) * 289 + (jj ? jj : 288);
#pragma unroll
            for (int i = 0; i < 8; ++i) { const int k = k0 + 64 * 289 * (16 * (i >> 2) + (i & 3)); o[u][i] = wr[k] | ((unsigned)wi[k] << 16); }
            const int q = jj / 24, c = jj - 24 * q;
            const int sg = c > 12 ? 24 - c : c == 12 ? 0 : c, dd = c > 12 ? 23 - q : c == 12 ? 12 + q : q;
            const int ww = sg / 3, tau = 3 * (sg - 3 * ww) + (dd >> 3), gg = (dd >> 1) & 3;
            const int ch = tau < 8 ? 4 * (16 * ww + 4 * (tau >> 1) + gg) + 2 * (tau & 1) + (dd & 1) : 256 + 8 * ww + 2 * gg + (dd & 1);
            d[u] = ws + O_WT + ((size_t)ch * 256 + t) * 32;
        }
#pragma unroll
        for (int u = 0; u < 2; ++u) { *(u32x4*)d[u] = u32x4{o[u][0], o[u][1], o[u][2], o[u][3]}; *(u32x4*)(d[u] + 16) = u32x4{o[u][4], o[u][5], o[u][6], o[u][7]}; }
    } else {
        if (bx == B_W2) { *(unsigned*)(ws + O_FLG + tid * 128) = 0u; *(unsigned*)(ws + O_FLG + 32768 + tid * 128) = 0u; }
        u32x4 o[2]; int kb[2], n[2];
#pragma unroll
        for (int u = 0; u < 2; ++u) {
            const int co = (bx - B_W2) * 512 + u * 256 + tid; kb[u] = co / 576; n[u] = co - kb[u] * 576;
#pragma unroll
            for (int i = 0; i < 4; ++i) o[u][i] = wo[ofeat(kb[u], 2 * i) * D + n[u]] | ((unsigned)wo[ofeat(kb[u], 2 * i + 1) * D + n[u]] << 16);
        }
#pragma unroll
        for (int u = 0; u < 2; ++u) *(u32x4*)(ws + O_WO2 + gimg_off(kb[u], n[u])) = o[u];
    }
}

__device__ __forceinline__ u32x4 neg8(u32x4 v) { return v ^ 0x80008000u; }
__global__ __launch_bounds__(512) void k_fft(const char* __restrict__ xin, char* __restrict__ xout, const char* __restrict__ ssp, const char* __restrict__ wt) {
    __shared__ __attribute__((aligned(16))) char lds[32768];
    const int tid = threadIdx.x, lane = tid & 63, w8 = __builtin_amdgcn_readfirstlane(tid >> 6), w = w8 & 3, z = w8 >> 2, col = lane & 15, g = lane >> 4;
    const int bx = blockIdx.x, kb = (bx & 7) * 9 + (bx >> 4), half = (bx >> 3) & 1;
    const bool dc = bx == 0;
    const int n0 = 32 * (16 * w + 4 * g) + 16 * z + col;
    const size_t cell0 = ((size_t)kb * S + n0) * 16 + half * 8;
    u32x2 xo[4]; u32x4 ss[4];
#pragma unroll
    for (int i = 0; i < 4; ++i) { xo[i] = *(const u32x2*)(xin + cell0 + i * 512); ss[i] = *(const u32x4*)(ssp + n0 * 16 + i * 512); }
    const char* wp = wt + ((size_t)(4 * kb + 2 * half + z) * 256 + (tid & 255)) * 32;
    const u32x4 w0 = *(const u32x4*)wp, w1 = *(const u32x4*)(wp + 16);
    u32x4 f64r[2], f64i[2], f32r[2], f32i[2];
    float t1c[8], t1s[8], t2c[8], t2s[8];
    {
        const float k1 = (float)(16 * w + col), fc = (float)col;
        float c[4], s[4];
#pragma unroll
        for (int i = 0; i < 4; ++i) { const float a = (float)(4 * g + i) * k1 * (1.f / 64.f); c[i] = __builtin_amdgcn_cosf(a); s[i] = -__builtin_amdgcn_sinf(a); }
        const unsigned odd = (col & 1) ? 0x80008000u : 0u;
        {
            const unsigned r0 = pk(c[0], c[1]), r1 = pk(c[2], c[3]), i0 = pk(s[0], s[1]), i1 = pk(s[2], s[3]);
            const unsigned sr = (col & 2) ? 0x80008000u : 0u, si = sr ^ odd;
            f64r[0] = u32x4{r0, r1, ((col & 1) ? i0 : r0) ^ sr, ((col & 1) ? i1 : r1) ^ sr};
            f64i[0] = u32x4{i0, i1, ((col & 1) ? r0 : i0) ^ si, ((col & 1) ? r1 : i1) ^ si};
            f64r[1] = f64r[0] ^ odd; f64i[1] = f64i[0] ^ odd;
        }
#pragma unroll
        for (int i = 0; i < 4; ++i) { const float a = (float)(4 * g + i) * fc * (1.f / 32.f); c[i] = __builtin_amdgcn_cosf(a); s[i] = -__builtin_amdgcn_sinf(a); }
        {
            const unsigned r0 = pk(c[0], c[1]), r1 = pk(c[2], c[3]), i0 = pk(s[0], s[1]), i1 = pk(s[2], s[3]);
            f32r[0] = u32x4{r0, r1, r0 ^ odd, r1 ^ odd}; f32i[0] = u32x4{i0, i1, i0 ^ odd, i1 ^ odd};
            f32r[1] = f32r[0] ^ 0x80000000u; f32i[1] = f32i[0] ^ 0x80000000u;
        }
#pragma unroll
        for (int i = 0; i < 8; ++i) {
            const float a = (float)(16 * (i >> 2) + 4 * g + (i & 3)) * k1 * (1.f / 2048.f);
            t1c[i] = __builtin_amdgcn_cosf(a); t1s[i] = __builtin_amdgcn_sinf(a);
            const float b = (float)(16 * (i >> 2) + col) * (float)(16 * w + 4 * g + (i & 3)) * (1.f / 2048.f);
            t2c[i] = __builtin_amdgcn_cosf(b); t2s[i] = __builtin_amdgcn_sinf(b);
        }
    }
    char* xw = lds + (w >> 1) * 4096 + lane * 16 + (w & 1) * 8;
    {
        float rho[4];
#pragma unroll
        for (int i = 0; i < 4; ++i) {
            float q = 0.f;
#pragma unroll
            for (int k = 0; k < 4; ++k) q = sum2(ss[i][k], q);
            rho[i] = __builtin_amdgcn_rsqf(q * (1.f / 576.f) + 1e-5f);
        }
#pragma unroll
        for (int c = 0; c < 2; ++c) {
            u32x2 re = {pk(blo(xo[0][c]) * rho[0], blo(xo[1][c]) * rho[1]), pk(blo(xo[2][c]) * rho[2], blo(xo[3][c]) * rho[3])};
            u32x2 im = {pk(bhi(xo[0][c]) * rho[0], bhi(xo[1][c]) * rho[1]), pk(bhi(xo[2][c]) * rho[2], bhi(xo[3][c]) * rho[3])};
            if (c == 0 && dc) { re = im; im = u32x2{0u, 0u}; }
            *(LAS u32x2*)(xw + c * 8192 + z * 2048) = re; *(LAS u32x2*)(xw + c * 8192 + z * 2048 + 1024) = im;
        }
    }
    __syncthreads();
    const char* xr = lds + lane * 16;
    f32x4 yr[2] = {}, yi[2] = {};
#pragma unroll
    for (int t = 0; t < 2; ++t)
#pragma unroll
        for (int s = 0; s < 2; ++s) {
            const bf16x8 ar = asb(lds128(xr + z * 8192 + s * 4096 + t * 2048)), ai = asb(lds128(xr + z * 8192 + s * 4096 + t * 2048 + 1024));
            yr[t] = MF16(ar, asb(f64r[s]), yr[t]); yr[t] = MF16(ai, asb(neg8(f64i[s])), yr[t]);
            yi[t] = MF16(ar, asb(f64i[s]), yi[t]); yi[t] = MF16(ai, asb(f64r[s]), yi[t]);
        }
    u32x4 br, bi;
    {
        float vr[8], vi[8];
#pragma unroll
        for (int i = 0; i < 8; ++i) { const float a = yr[i >> 2][i & 3], b = yi[i >> 2][i & 3]; vr[i] = a * t1c[i] + b * t1s[i]; vi[i] = b * t1c[i] - a * t1s[i]; }
#pragma unroll
        for (int q = 0; q < 4; ++q) { br[q] = pk(vr[2 * q], vr[2 * q + 1]); bi[q] = pk(vi[2 * q], vi[2 * q + 1]); }
    }
#pragma unroll
    for (int t = 0; t < 2; ++t) {
        yr[t] = MF16(asb(f32r[t]), asb(br), f32x4{}); yr[t] = MF16(asb(neg8(f32i[t])), asb(bi), yr[t]);
        yi[t] = MF16(asb(f32i[t]), asb(br), f32x4{}); yi[t] = MF16(asb(f32r[t]), asb(bi), yi[t]);
    }
    {
        float vr[8], vi[8];
#pragma unroll
        for (int i = 0; i < 8; ++i) { const unsigned wv = i < 4 ? w0[i & 3] : w1[i & 3]; const float c = blo(wv) * (1.f / 2048.f), d = bhi(wv) * (1.f / 2048.f);
            const float a = yr[i >> 2][i & 3], b = yi[i >> 2][i & 3]; vr[i] = a * c - b * d; vi[i] = a * d + b * c; }
#pragma unroll
        for (int q = 0; q < 4; ++q) { br[q] = pk(vr[2 * q], vr[2 * q + 1]); bi[q] = pk(vi[2 * q], vi[2 * q + 1]); }
    }
#pragma unroll
    for (int t = 0; t < 2; ++t) {
        yr[t] = MF16(asb(br), asb(f32r[t]), f32x4{}); yr[t] = MF16(asb(bi), asb(f32i[t]), yr[t]);
        yi[t] = MF16(asb(bi), asb(f32r[t]), f32x4{}); yi[t] = MF16(asb(br), asb(neg8(f32i[t])), yi[t]);
    }
    {
        float vr[8], vi[8];
#pragma unroll
        for (int i = 0; i < 8; ++i) { const float a = yr[i >> 2][i & 3], b = yi[i >> 2][i & 3]; vr[i] = a * t2c[i] - b * t2s[i]; vi[i] = b * t2c[i] + a * t2s[i]; }
#pragma unroll
        for (int t = 0; t < 2; ++t) {
            *(LAS u32x2*)(xw + 16384 + z * 8192 + t * 2048) = u32x2{pk(vr[4 * t], vr[4 * t + 1]), pk(vr[4 * t + 2], vr[4 * t + 3])};
            *(LAS u32x2*)(xw + 16384 + z * 8192 + t * 2048 + 1024) = u32x2{pk(vi[4 * t], vi[4 * t + 1]), pk(vi[4 * t + 2], vi[4 * t + 3])};
        }
    }
    __syncthreads();
    yr[0] = f32x4{}; yr[1] = f32x4{}; yi[0] = f32x4{}; yi[1] = f32x4{};
#pragma unroll
    for (int c = 0; c < 2; ++c)
#pragma unroll
        for (int s = 0; s < 2; ++s) {
            const bf16x8 ur = asb(lds128(xr + 16384 + c * 8192 + s * 4096 + z * 2048)), ui = asb(lds128(xr + 16384 + c * 8192 + s * 4096 + z * 2048 + 1024));
            yr[c] = MF16(asb(f64r[s]), ur, yr[c]); yr[c] = MF16(asb(f64i[s]), ui, yr[c]);
            yi[c] = MF16(asb(f64r[s]), ui, yi[c]); yi[c] = MF16(asb(neg8(f64i[s])), ur, yi[c]);
        }
    if (dc) { yi[0] = yr[0]; yr[0] = f32x4{}; }
#pragma unroll
    for (int i = 0; i < 4; ++i)
        *(u32x2*)(xout + cell0 + i * 512) = u32x2{pk(blo(xo[i][0]) + yr[0][i], bhi(xo[i][0]) + yi[0][i]), pk(blo(xo[i][1]) + yr[1][i], bhi(xo[i][1]) + yi[1][i])};
}

constexpr float QSCALE = 0.23570226039551584f * 1.4426950408889634f;
#define EX(x) __builtin_amdgcn_exp2f(x)
__device__ __forceinline__ float xhalf_max(float m) {
    auto rr = __builtin_amdgcn_permlane32_swap(__float_as_uint(m), __float_as_uint(m), false, false);
    return fmaxf(__uint_as_float(rr[0]), __uint_as_float(rr[1]));
}
__device__ __forceinline__ float max16(const f32x16& s) {
    float a = fmaxf(fmaxf(s[0], s[1]), s[2]), b = fmaxf(fmaxf(s[3], s[4]), s[5]);
    a = fmaxf(fmaxf(a, s[6]), s[7]); b = fmaxf(fmaxf(b, s[8]), s[9]); a = fmaxf(fmaxf(a, s[10]), s[11]); b = fmaxf(fmaxf(b, s[12]), s[13]);
    a = fmaxf(fmaxf(a, s[14]), s[15]);
    return xhalf_max(fmaxf(a, b));
}
constexpr int ATT_CONST = 8 * (int)KVCH;
constexpr int L_W = 3 * (int)KVCH, L_F2 = L_W + 65536, L_SC = (int)KVCH, L_CNT = ATT_CONST + 10240;
#define ST(i)
#define STP(n)
#define A_XP(S, D) "v_exp_f32 v[" D "], v[" S "]\n v_exp_f32 v[" D "+1], v[" S "+1]\n v_exp_f32 v[" D "+2], v[" S "+2]\n v_exp_f32 v[" D "+3], v[" S "+3]\n"
#define A_QK1(SN, KC) "v_mfma_f32_32x32x16_bf16 v[" SN ":" SN "+15], v[" KC ":" KC "+3], v[76:79], 0\n"
#define A_QK2(SN, K2, QN) "v_mfma_f32_32x32x8bf16_1k v[" SN ":" SN "+15], v[" K2 ":" K2 "+1], v[" QN ":" QN "+1], v[" SN ":" SN "+15]\n"
#define A_PV(V, P) "v_smfmac_f32_32x32x32_bf16 v[0:15], v[" V ":" V "+3], v[" P ":" P "+7], v84\n"
#define A_R128(D, A, O) "ds_read_b128 v[" D ":" D "+3], " A " offset:" O "\n"
#define A_R32(D, O) "ds_read_b32 v" D ", v86 offset:" O "\n"
#define A_STEP(SC, Q1, Q2, LD) Q1 A_XP(SC, SC) Q2 A_XP(SC "+4", SC "+4") "s_waitcnt lgkmcnt(0)\n" A_PV("72", "48") A_XP(SC "+8", "48") A_PV("68", SC) LD A_XP(SC "+12", "52")
#define A_VV(T) A_R128("72", "v87", #T "*1152+576") A_R128("68", "v87", #T "*1152+1152")
#define A_S0(H) A_STEP("16", A_QK1("32", "60"), A_QK2("32", "64", "82"), A_VV(0) A_R128("60", "v85", "3072") H)
#define A_S1(H) A_STEP("32", A_QK1("16", "56"), A_QK2("16", "66", "80"), A_VV(1) A_R128("56", "v85", "4096") A_R32("64", "512") H)
#define A_S2(H) A_STEP("16", A_QK1("32", "60"), A_QK2("32", "66", "82"), A_VV(2) A_R128("60", "v85", "5120") H)
#define A_S3(H) A_STEP("32", A_QK1("16", "56"), A_QK2("16", "64", "80"), A_VV(3) A_R128("56", "v85", "6144") A_R32("66", "768") H)
#define A_S4(H) A_STEP("16", A_QK1("32", "60"), A_QK2("32", "64", "82"), A_VV(4) A_R128("60", "v85", "7168") H)
#define A_S5(H) A_STEP("32", A_QK1("16", "56"), A_QK2("16", "66", "80"), A_VV(5) H)
#define A_S6(H) A_STEP("16", A_QK1("32", "60"), A_QK2("32", "66", "82"), A_VV(6) H)
#define A_NEXT "v_add_u32 v87, v87, v88\n v_add_u32 v85, 0x4800, v85\n v_add_u32 v86, 0x4800, v86\n"
#define A_S567 A_S5(A_R128("56", "v85", "18432") A_R32("64", "18432")) A_S6(A_R128("60", "v85", "19456")) \
    A_STEP("32", A_QK1("16", "56"), A_QK2("16", "64", "80"), A_R128("72", "v87", "8640") A_NEXT A_R128("68", "v87", "0") A_R128("56", "v85", "2048") A_R32("66", "256"))
#define A_PRIME A_R128("56", "v85", "0") A_R32("64", "0") A_R128("60", "v85", "1024") A_R32("66", "256") A_R128("68", "v87", "0") \
    "s_waitcnt lgkmcnt(3)\n" A_QK1("16", "56") A_QK2("16", "64", "80") A_R128("56", "v85", "2048") "s_waitcnt lgkmcnt(1)\n s_nop 10\n"
#define A_SIGNAL(L) "s_waitcnt vmcnt(0)\n s_mov_b64 exec, 1\n v_mov_b32 v93, %[cnt]\n v_mov_b32 v94, 1\n v_mov_b32 v95, %[mine]\n ds_add_rtn_u32 v92, v93, v94\n" \
    "s_waitcnt lgkmcnt(0)\n v_readfirstlane_b32 %[tmp], v92\n s_nop 3\n s_cmp_lg_u32 %[tmp], 7\n s_cbranch_scc1 " L "f\n global_store_dword v95, v94, %[fb] sc1\n" \
    L ":\n s_mov_b64 exec, -1\n"
#define A_POLL(L, TEST) "s_mov_b32 %[tmp], 0x200000\n" L ":\n global_load_dword v112, v90, %[fb] sc1\n s_waitcnt vmcnt(0)\n v_cmp_eq_u32 vcc, 1, v112\n s_nop 3\n" \
    TEST "s_cbranch_scc1 " L "0f\n s_sleep 1\n s_sub_u32 %[tmp], %[tmp], 1\n s_cbranch_scc0 " L "b\n" L "0:\n"
#define A_GL(M) "buffer_load_dwordx4 v[92+4*" #M ":95+4*" #M "], v89, %[rk], %[so] offen sc1\n s_add_u32 %[so], %[so], 0x2000\n s_cmp_ge_u32 %[so], 0x24000\n" \
    "s_cselect_b32 %[tmp], 0x24000, 0\n s_sub_u32 %[so], %[so], %[tmp]\n"
#define A_WR(M, A, O) "ds_write_b128 " A ", v[92+4*" #M ":95+4*" #M "] offset:" O "\n"
#define A_BAR "s_waitcnt lgkmcnt(0)\n s_barrier\n"

constexpr int QLA = 12;
template <bool LAST> constexpr int q_wait(int s) {
    const int hi = s + QLA < 35 ? s + QLA : 35; int n = hi - s;
    for (int q = 1; q < 4; ++q) if (9 * q - 1 > s && 9 * q - 1 <= hi) n += (LAST && q < 3) ? 3 : 2;
    return n;
}
template <bool LAST>
__global__ __launch_bounds__(512) void k_qa(const char* __restrict__ xh, const char* __restrict__ wqh, char* kv, unsigned* flags, char* __restrict__ o,
                                            char* __restrict__ x3, char* dbg) {
    __shared__ __attribute__((aligned(256))) char lds[L_CNT + 16];
    const int tid = threadIdx.x, lane = tid & 63, w = __builtin_amdgcn_readfirstlane(tid >> 6), r = lane & 31, hh = lane >> 5;
    const int bx = blockIdx.x, xcd = bx & 7, idx = bx >> 3, b = (xcd >> 1) * 2 + (idx & 1), h = (xcd & 1) * 16 + (idx >> 1);
    const int row = b * 256 + w * 32 + r;
    const unsigned lb = (unsigned)(uintptr_t)lds;
    ST(0);
    u32x4 xf[36];
    const char* wsrc = wqh + (size_t)h * HIMG + w * 1024 + lane * 16;
    const char* f2 = (const char*)&F2H + (size_t)h * 20736;
    const rsrc_t rx = mkrsrc(xh, (int)CELLS);
    const int xo = (hh * S + row) * 16;
    auto issue = [&](int s) {
        if (s == 0 || s == 8 || s == 17 || s == 26) {
            const int q = (s + 1) / 9;
            glds16(wsrc + (2 * q) * 8192, lb + L_W + w * 1024 + (2 * q) * 8192);
            glds16(wsrc + (2 * q + 1) * 8192, lb + L_W + w * 1024 + (2 * q + 1) * 8192);
            if (LAST && q < 3) { const int of = (q * 8 + w) * 1024 + lane * 16; glds16(f2 + (of < 20736 ? of : 0), lb + L_F2 + w * 1024 + q * 8192); }
        }
        asm volatile("buffer_load_dwordx4 %0, %1, %2, %3 offen" : "=v"(xf[s]) : "v"(xo), "s"(rx), "s"(s * (2 * S * 16)));
    };
#pragma unroll
    for (int s = 0; s < QLA; ++s) issue(s);
    if (tid < 128) { const unsigned v = (tid & 1) ? 0u : 0x3F803F80u; *(LAS u32x4*)(lds + ATT_CONST + (tid >> 2) * 288 + (2 + (tid & 3)) * 16) = u32x4{v, v, v, v}; }
    if (tid == 128) *(LAS unsigned*)(lds + L_CNT) = 0u;
    f32x16 aa = {}, ab = {}, ac = {};
    float q0 = 0.f, q1 = 0.f;
    const char* pa = lds + L_W + lane * 16;
    const char* pb = lds + L_W + 1024 + hh * 352 + (r < 22 ? r : 21) * 16;
    const char* pc = lds + L_F2 + hh * 288 + (r < 18 ? r : 17) * 16;
    asm volatile("s_waitcnt vmcnt(%0)\n\ts_barrier" :: "n"(q_wait<LAST>(0)) : "memory");
    ST(1);
    u32x4 wa = lds128(pa), wb = lds128(pb), wc = {};
    if (LAST) wc = lds128(pc);
#pragma unroll
    for (int s = 0; s < 36; ++s) {
        if (s + QLA < 36) issue(s + QLA);
        if (s == 8 || s == 17 || s == 26) asm volatile("s_waitcnt vmcnt(%1)\n\ts_barrier" : "+v"(xf[s]) : "n"(q_wait<LAST>(s)) : "memory");
        else asm volatile("s_waitcnt vmcnt(%1)" : "+v"(xf[s]) : "n"(q_wait<LAST>(s)));
        u32x4 na = wa, nb = wb, nc = wc;
        if (s < 35) { na = lds128(pa + (s + 1) * 1728); nb = lds128(pb + (s + 1) * 1728); if (LAST) nc = lds128(pc + (s + 1) * 576); }
        aa = MF32(asb(wa), asb(xf[s]), aa);
        q0 = sq2((s == 0 && hh == 0) ? xf[s][0] & 0xFFFF0000u : xf[s][0] + 0u, q0); q1 = sq2(xf[s][1] + 0u, q1);
        ab = MF32(asb(xf[s]), asb(wb), ab);
        q0 = sq2(xf[s][2] + 0u, q0); q1 = sq2(xf[s][3] + 0u, q1);
        if (LAST) ac = MF32(asb(wc), asb(xf[s]), ac);
        wa = na; wb = nb; wc = nc;
        SB();
    }
    ST(2);
    q0 += q1;
    { auto t = __builtin_amdgcn_permlane32_swap(__float_as_uint(q0), __float_as_uint(q0), false, false); q0 = __uint_as_float(t[0]) + __uint_as_float(t[1]); }
    const float rho = __builtin_amdgcn_rsqf(q0 * (1.f / 576.f) + 1e-5f);
    float* sc = (float*)(lds + L_SC + w * 640);
    if (hh == 0) *(LAS float*)(sc + r) = rho;
    if (r >= 18 && r < 22) {
#pragma unroll
        for (int i = 0; i < 4; ++i) *(LAS f32x4*)(sc + (r - 17) * 32 + 8 * i + 4 * hh) = f32x4{ab[4 * i], ab[4 * i + 1], ab[4 * i + 2], ab[4 * i + 3]};
    }
    const float rq = rho * QSCALE;
    const rsrc_t rk = mkrsrc(kv + (size_t)h * 8 * KVCH, 8 * (int)KVCH);
    const int own = b * (int)KVCH, rp = (r & 19) | ((r & 4) << 1) | ((r & 8) >> 1);
    const u32x4 qf = {pk(aa[0] * rq, aa[1] * rq), pk(aa[2] * rq, aa[3] * rq), pk(aa[4] * rq, aa[5] * rq), pk(aa[6] * rq, aa[7] * rq)};
    const u32x4 kf = {pk(aa[8] * rho, aa[9] * rho), pk(aa[10] * rho, aa[11] * rho), pk(aa[12] * rho, aa[13] * rho), pk(aa[14] * rho, aa[15] * rho)};
    __builtin_amdgcn_raw_buffer_store_b128(kf, rk, w * 1024 + hh * 512 + rp * 16, own, 16);
    asm volatile("s_waitcnt lgkmcnt(0)" ::: "memory");
    f32x4 rr[4];
#pragma unroll
    for (int i = 0; i < 4; ++i) rr[i] = *(const LAS f32x4*)(sc + 8 * i + 4 * hh);
    const float l0 = *(const LAS float*)(sc + 32 + r), l1 = *(const LAS float*)(sc + 64 + r), l2 = *(const LAS float*)(sc + 96 + r), l3 = *(const LAS float*)(sc + 128 + r);
    *(LAS u32x4*)(lds + w * 1024 + hh * 512 + rp * 16) = kf;
    if (r < 18) {
#pragma unroll
        for (int s = 0; s < 2; ++s) {
            const u32x4 vf = {pk(ab[8 * s] * rr[2 * s][0], ab[8 * s + 1] * rr[2 * s][1]), pk(ab[8 * s + 2] * rr[2 * s][2], ab[8 * s + 3] * rr[2 * s][3]),
                              pk(ab[8 * s + 4] * rr[2 * s + 1][0], ab[8 * s + 5] * rr[2 * s + 1][1]), pk(ab[8 * s + 6] * rr[2 * s + 1][2], ab[8 * s + 7] * rr[2 * s + 1][3])};
            const int vo = 9216 + w * 1152 + s * 576 + hh * 288 + r * 16;
            __builtin_amdgcn_raw_buffer_store_b128(vf, rk, vo, own, 16);
            *(LAS u32x4*)(lds + vo) = vf;
        }
    }
    const unsigned q67 = pk(l0 * rq, l1 * rq), k67 = pk(l2 * rho, l3 * rho);
    if (hh) {
        __builtin_amdgcn_raw_buffer_store_b32(k67, rk, 8192 + (w * 32 + rp) * 4, own, 16);
        *(LAS unsigned*)(lds + 8192 + (w * 32 + rp) * 4) = k67;
    }
    if (LAST) {
        *(u32x4*)(x3 + ((size_t)(2 * h + hh) * S + row) * 16) = u32x4{pk(ac[0], ac[1]), pk(ac[2], ac[3]), pk(ac[4], ac[5]), pk(ac[6], ac[7])};
        if (hh == 0) *(unsigned*)(x3 + ((size_t)(64 + (h >> 2)) * S + row) * 16 + (h & 3) * 4) = pk(ac[8], ac[9]);
    }
    u32x2 qa = {hh ? 0u : q67, 0u}, qb = {hh ? q67 : 0u, 0u};
    {
        f32x16 s0 = MF32(asb(kf), asb(qf), f32x16{});
        s0 = MF32(asb(u32x4{k67, 0u, 0u, 0u}), asb(u32x4{qa[0], 0u, 0u, 0u}), s0);
        const unsigned nm = pk(-max16(s0), 0.f) & 0xFFFFu;
        qa[1] = hh ? 0u : nm; qb[1] = hh ? nm : 0u;
    }
    const bool isv = r < 18;
    const int cslot = ((r >= 20 && r < 28) ? 4 : 2) + ((r == 18 || r == 20) ? 0 : 1);
    const int vo0 = isv ? 9216 + hh * 288 + r * 16 : ATT_CONST + hh * 288 + cslot * 16, vstep = isv ? (int)KVCH : 0;
    f32x16 acc;
    {
        unsigned kp = lb + lane * 16, k2p = lb + 8192 + lane * 4, vp = lb + vo0;
        unsigned foff = ((b + 1 + lane) & 7) * 128;
        int so = (((b + 1) & 7) * (int)KVCH) + w * 1024, tmp, n = 5;
        asm volatile(
            "v_mov_b32 v84, 0xdddddddd\n v_mov_b32 v65, 0x3f80\n v_mov_b32 v67, 0x3f80\n"
            ".irp r,0,1,2,3,4,5,6,7,8,9,10,11,12,13,14,15,48,49,50,51,52,53,54,55,72,73,74,75\n v_mov_b32 v\\r, 0\n .endr\n"
            "v_subrev_u32 v89, %[lb], v85\n v_add_u32 v91, %[pd], v85\n"
            A_BAR STP(0)
            "s_cmp_lt_u32 %[w], 4\n s_cbranch_scc1 20f\n" A_SIGNAL("21") "s_branch 22f\n 20:\n s_setprio 1\n 22:\n"
            A_PRIME A_S0() A_S1() A_S2() A_S3() A_S4()
            "s_cmp_lt_u32 %[w], 4\n s_cbranch_scc0 23f\n" A_SIGNAL("24") "23:\n"
            A_S5() A_S6() A_STEP("32", , , A_R128("72", "v87", "8640") A_NEXT) STP(1)
            "s_cmp_lt_u32 %[w], 4\n s_cbranch_scc0 25f\n s_setprio 0\n"
            A_POLL("3", "s_bitcmp1_b32 vcc_lo, 0\n") STP(2)
            "buffer_load_dwordx4 v[92:95], v89, %[rk], %[so] offen sc1\n s_add_u32 %[tmp], %[so], 0x1000\n"
            "buffer_load_dwordx4 v[96:99], v89, %[rk], %[tmp] offen sc1\n s_add_u32 %[tmp], %[so], 0x2000\n"
            "buffer_load_dwordx4 v[100:103], v89, %[rk], %[tmp] offen sc1\n s_add_u32 %[tmp], %[so], 0x3000\n"
            "buffer_load_dwordx4 v[104:107], v89, %[rk], %[tmp] offen sc1\n s_lshl_b32 %[tmp], %[w], 9\n s_sub_u32 %[tmp], 0x4000, %[tmp]\n"
            "v_lshrrev_b32 v110, 1, v89\n v_sub_u32 v111, v91, v110\n v_add_u32 v111, %[tmp], v111\n s_add_u32 %[tmp], %[so], %[tmp]\n"
            "buffer_load_dwordx2 v[108:109], v110, %[rk], %[tmp] offen sc1\n"
            "s_cmp_eq_u32 vcc_lo, -1\n s_cbranch_scc1 40f\n"
            A_POLL("4", "s_cmp_eq_u32 vcc_lo, -1\n")
            "s_waitcnt vmcnt(4)\n" A_WR(0, "v91", "0") "s_waitcnt vmcnt(3)\n" A_WR(1, "v91", "4096") "s_waitcnt vmcnt(2)\n" A_WR(2, "v91", "8192")
            "s_waitcnt vmcnt(1)\n" A_WR(3, "v91", "12288") "s_waitcnt vmcnt(0)\n ds_write_b64 v111, v[108:109]\n"
            "25:\n"
            "s_add_u32 %[so], %[so], 0x4800\n s_cmp_ge_u32 %[so], 0x24000\n s_cselect_b32 %[tmp], 0x24000, 0\n s_sub_u32 %[so], %[so], %[tmp]\n"
            "v_add_u32 v90, 0x10000, v91\n"
            A_BAR STP(3)
            A_GL(0) A_GL(1) A_GL(2)
            A_PRIME A_S0(A_GL(3) A_GL(4) A_GL(5)) A_S1(A_GL(6) A_GL(7) A_GL(8)) A_S2(A_GL(9) A_GL(10) A_GL(11)) A_S3(A_GL(12) A_GL(13))
            A_S4("s_waitcnt vmcnt(11)\n" A_WR(0, "v91", "18432") A_WR(1, "v91", "18432+8192") A_WR(2, "v91", "18432+2*8192"))
            A_BAR STP(4) A_S567
            A_S0() A_S1("s_waitcnt vmcnt(8)\n" A_WR(3, "v91", "18432+3*8192") A_WR(4, "v91", "18432+4*8192") A_WR(5, "v91", "18432+5*8192"))
            A_S2("s_waitcnt vmcnt(5)\n" A_WR(6, "v90", "18432+6*8192-65536") A_WR(7, "v90", "18432+7*8192-65536") A_WR(8, "v90", "18432+8*8192-65536"))
            A_S3("s_waitcnt vmcnt(2)\n" A_WR(9, "v90", "18432+9*8192-65536") A_WR(10, "v90", "18432+10*8192-65536") A_WR(11, "v90", "18432+11*8192-65536"))
            A_S4("s_waitcnt vmcnt(0)\n" A_WR(12, "v90", "18432+12*8192-65536") "s_cmp_lt_u32 %[w], 4\n s_cbranch_scc0 26f\n" A_WR(13, "v90", "18432+13*8192-65536") "26:\n")
            A_BAR STP(5) A_S567
            "27:\n" A_S0() A_S1() A_S2() A_S3() A_S4() A_S567
            "s_sub_u32 %[n], %[n], 1\n s_cmp_lg_u32 %[n], 0\n s_cbranch_scc1 27b\n"
            "s_waitcnt lgkmcnt(0)\n" A_PV("72", "48") "s_nop 15\n"
            : "={v[0:15]}"(acc), "+{v85}"(kp), "+{v86}"(k2p), "+{v87}"(vp), "+{v90}"(foff), [so] "+s"(so), [tmp] "=&s"(tmp), [n] "+s"(n)
            : "{v[76:79]}"(qf), "{v[80:81]}"(qa), "{v[82:83]}"(qb), "{v88}"(vstep), [rk] "s"(rk), [fb] "s"(flags + h * 256), [w] "s"(w),
              [lb] "s"(lb), [pd] "s"((int)KVCH + w * 1024), [cnt] "s"(lb + L_CNT), [mine] "s"(b * 128)
            : "memory", "scc", "vcc", "v16", "v17", "v18", "v19", "v20", "v21", "v22", "v23", "v24", "v25", "v26", "v27", "v28", "v29", "v30", "v31", "v32", "v33", "v34", "v35",
              "v36", "v37", "v38", "v39", "v40", "v41", "v42", "v43", "v44", "v45", "v46", "v47", "v48", "v49", "v50", "v51", "v52", "v53", "v54", "v55", "v56", "v57", "v58",
              "v59", "v60", "v61", "v62", "v63", "v64", "v65", "v66", "v67", "v68", "v69", "v70", "v71", "v72", "v73", "v74", "v75", "v84", "v89", "v91", "v92", "v93", "v94",
              "v95", "v96", "v97", "v98", "v99", "v100", "v101", "v102", "v103", "v104", "v105", "v106", "v107", "v108", "v109", "v110", "v111", "v112", "v113", "v114", "v115",
              "v116", "v117", "v118", "v119", "v120", "v121", "v122", "v123", "v124", "v125", "v126", "v127", "v128", "v129", "v130", "v131", "v132", "v133", "v134", "v135",
              "v136", "v137", "v138", "v139", "v140", "v141", "v142", "v143", "v144", "v145", "v146", "v147");
    }
    ST(9);
    float l = hh ? acc[8] : acc[10];
    bool ok = l > 1e-12f && l < 1e30f;
#pragma unroll
    for (int i = 0; i < 10; ++i) ok = ok && (fabsf(acc[i]) < 3e38f);
    if (__builtin_expect(!__all(ok), 0)) {
        acc = f32x16{};
        float m = -3.0e38f;
        for (int t = 0; t < 64; ++t) {
            const int co = (t >> 3) * (int)KVCH, tt = t & 7;
            const unsigned k2 = *(const LAS unsigned*)(lds + 8192 + lane * 4 + co + (tt >> 1) * 256);
            f32x16 sA = MF32(asb(lds128(lds + lane * 16 + co + tt * 1024)), asb(qf), f32x16{});
            sA = MF32(asb(u32x4{k2, 0u, 0u, 0u}), asb(u32x4{(tt & 1) ? qb[0] : qa[0], 0u, 0u, 0u}), sA);
            const float mn = fmaxf(m, max16(sA)), al = EX(m - mn);
            m = mn;
#pragma unroll
            for (int i = 0; i < 16; ++i) acc[i] *= al;
            f32x8 p0, p1;
#pragma unroll
            for (int i = 0; i < 8; ++i) { p0[i] = EX(sA[i] - mn); p1[i] = EX(sA[8 + i] - mn); }
            const char* vq = lds + vo0 + (t >> 3) * vstep + tt * 1152;
            acc = __builtin_amdgcn_smfmac_f32_32x32x32_bf16(asb(lds128(vq)), __builtin_bit_cast(bf16x16, p0), acc, (int)0xDDDDDDDDu, 0, 0);
            acc = __builtin_amdgcn_smfmac_f32_32x32x32_bf16(asb(lds128(vq + 576)), __builtin_bit_cast(bf16x16, p1), acc, (int)0xDDDDDDDDu, 0, 0);
        }
        l = hh ? acc[8] : acc[10];
    }
    const float il = 1.f / l;
    *(u32x4*)(o + ((size_t)(2 * h + hh) * S + row) * 16) = u32x4{pk(acc[0] * il, acc[1] * il), pk(acc[2] * il, acc[3] * il), pk(acc[4] * il, acc[5] * il), pk(acc[6] * il, acc[7] * il)};
    if (hh == 0) *(unsigned*)(o + ((size_t)(64 + (h >> 2)) * S + row) * 16 + (h & 3) * 4) = pk(acc[8] * il, acc[9] * il);
    ST(10);
}

template <bool LAST>
__global__ __launch_bounds__(256) void k_oproj(const char* __restrict__ o, const char* __restrict__ wimg, const char* __restrict__ res, char* __restrict__ out, char* __restrict__ ssp) {
    __shared__ __attribute__((aligned(16))) char lds[86016];
    const int tid = threadIdx.x, lane = tid & 63, wid = __builtin_amdgcn_readfirstlane(tid >> 6), bx = blockIdx.x;
    const int idx = bx >> 3, rb = (bx & 7) * 4 + (idx >> 3), a = idx & 7, p = lane & 15, g = lane >> 4, r = rb * 64 + wid * 16 + p;
    u32x4 rf[18];
    const u32x4 e0 = *(const u32x4*)(res + ((size_t)(9 * a + g) * S + r) * 16), e1 = *(const u32x4*)(res + ((size_t)(9 * a + 4 + g) * S + r) * 16);
    const u32x2 e2 = *(const u32x2*)(res + ((size_t)(9 * a + 8) * S + r) * 16 + (g & 1) * 8);
#pragma unroll
    for (int s = 0; s < 18; ++s) rf[s] = *(const u32x4*)(o + ((size_t)(4 * s + g) * S + r) * 16);
    gemm72_issue(wimg + a * GIMG, lds, wid, lane);
    f32x4 acc[5] = {{blo(e0[0]), bhi(e0[0]), blo(e0[1]), bhi(e0[1])}, {blo(e0[2]), bhi(e0[2]), blo(e0[3]), bhi(e0[3])},
                    {blo(e1[0]), bhi(e1[0]), blo(e1[1]), bhi(e1[1])}, {blo(e1[2]), bhi(e1[2]), blo(e1[3]), bhi(e1[3])},
                    {blo(e2[0]), bhi(e2[0]), blo(e2[1]), bhi(e2[1])}};
    gemm72_run(lds, lane, rf, acc);
    if (!LAST) store_xh(acc, out, ssp, a, r, g);
    else {
        char* d = out + ((size_t)r * D + 72 * a) * 2;
#pragma unroll
        for (int u = 0; u < 2; ++u)
            *(u32x4*)(d + (32 * u + 8 * g) * 2) = u32x4{pk(acc[2 * u][0], acc[2 * u][1]), pk(acc[2 * u][2], acc[2 * u][3]), pk(acc[2 * u + 1][0], acc[2 * u + 1][1]), pk(acc[2 * u + 1][2], acc[2 * u + 1][3])};
        if (g < 2) *(u32x2*)(d + 128 + g * 8) = u32x2{pk(acc[4][0], acc[4][1]), pk(acc[4][2], acc[4][3])};
    }
}

extern "C" void kernel_launch(void* const* d_in, const int* in_sizes, int n_in, void* d_out, int out_size, void* d_ws, size_t ws_size, hipStream_t stream) {
    const u16* x = (const u16*)d_in[0]; const u16* wr = (const u16*)d_in[1]; const u16* wi = (const u16*)d_in[2];
    const u16* wqkv = (const u16*)d_in[4]; const u16* wo = (const u16*)d_in[5];
    char* ws = (char*)d_ws;
    unsigned* fl = (unsigned*)(ws + O_FLG);
    k_in<<<B_END, 256, 0, stream>>>(x, wr, wi, wqkv, wo, ws);
    k_fft<<<144, 512, 0, stream>>>(ws + O_XH0, ws + O_XH1, ws + O_SS0, ws + O_WT);
    k_qa<false><<<256, 512, 0, stream>>>(ws + O_XH1, ws + O_WQH, ws + O_KVA, fl, ws + O_OA, ws + O_X3, ws + O_DBG);
    k_oproj<false><<<256, 256, 0, stream>>>(ws + O_OA, ws + O_WOH, ws + O_XH1, ws + O_XH2, ws + O_SS2);
    k_fft<<<144, 512, 0, stream>>>(ws + O_XH2, ws + O_XH3, ws + O_SS2, ws + O_WT);
    k_qa<true><<<256, 512, 0, stream>>>(ws + O_XH3, ws + O_WQH, ws + O_KVB, fl + 8192, ws + O_OB, ws + O_X3, ws + O_DBG + 262144);
    k_oproj<true><<<256, 256, 0, stream>>>(ws + O_OB, ws + O_WO2, ws + O_X3, (char*)d_out, ws + O_SS2);
}
```

```cpp
#include <hip/hip_runtime.h>
#include <hip/hip_bf16.h>
#include <stdint.h>
#include <utility>

typedef __attribute__((ext_vector_type(8))) __bf16 bf16x8;
typedef __attribute__((ext_vector_type(2))) __bf16 bf16x2;
typedef __attribute__((ext_vector_type(2))) float f32x2;
typedef __attribute__((ext_vector_type(4))) float f32x4;
typedef __attribute__((ext_vector_type(16))) float f32x16;
typedef __attribute__((ext_vector_type(8))) float f32x8;
typedef __attribute__((ext_vector_type(16))) __bf16 bf16x16;
typedef __attribute__((ext_vector_type(4))) unsigned u32x4;
typedef __attribute__((ext_vector_type(2))) unsigned u32x2;
typedef __attribute__((ext_vector_type(3))) unsigned u32x3;
typedef unsigned short u16;
typedef __attribute__((address_space(1))) unsigned gu32;
typedef __amdgpu_buffer_rsrc_t rsrc_t;
#define LAS __attribute__((address_space(3)))

constexpr int S = 2048, D = 576, NH = 32;

namespace tbl {
constexpr double PI = 3.14159265358979323846264338327950288;
constexpr double ccos(int m) {
    if (m > 288) m = 576 - m;
    double sg = 1.0;
    if (m > 144) { m = 288 - m; sg = -1.0; }
    if (m == 144) return 0.0;
    const double x = 2.0 * PI * m / 576.0, x2 = x * x;
    double term = 1.0, sum = 1.0;
    for (int i = 1; i < 16; ++i) { term *= -x2 / ((2 * i - 1) * (2 * i)); sum += term; }
    return sg * sum;
}
constexpr u16 tobf(double v) {
    const unsigned u = __builtin_bit_cast(unsigned, (float)v);
    return (u16)((u + 0x7FFFu + ((u >> 16) & 1u)) >> 16);
}
struct Base { u16 c[576], u[576]; };
constexpr double SC = 0.05892556509887896;
constexpr Base mkbase() { Base b{}; for (int m = 0; m < 576; ++m) { b.c[m] = tobf(SC * ccos(m)); b.u[m] = tobf(ccos(m)); } return b; }
constexpr Base base = mkbase();
constexpr u16 DCV = tobf(1.0 / 24.0), NDCV = tobf(-1.0 / 24.0);
constexpr int freq(int ch) {
    const int kb = ch >> 2, cc = ch & 3;
    const int w = kb < 64 ? kb >> 4 : (kb - 64) >> 1, g = kb < 64 ? kb & 3 : 2 * (kb & 1) + (cc >> 1), tau = kb < 64 ? 2 * ((kb >> 2) & 3) + (cc >> 1) : 8;
    const int sg = 3 * w + tau / 3, d = 8 * (tau % 3) + 2 * g + (cc & 1);
    if (sg) return d < 12 ? sg + 24 * d : 576 - sg - 24 * d;
    return d < 12 ? 24 * d : 12 + 24 * (d - 12);
}
constexpr u16 Fval(int n, int c) {
    if (c < 2) return (c == 0 || !(n & 1)) ? DCV : NDCV;
    return base.c[(freq(c >> 1) * n + ((c & 1) ? 144 : 0)) % 576];
}
struct Chunk { u16 v[4608]; };
template <int I> constexpr Chunk mkF2() {
    Chunk c{}; int o = 0;
    for (int q = 0; q < 16; ++q) { const int hs = I * 16 + q, h = hs / 36, s = hs % 36;
        for (int hh = 0; hh < 2; ++hh) for (int p = 0; p < 18; ++p) {
            const int n = p < 16 ? 16 * h + 8 * ((p >> 2) & 1) + (p & 3) + 4 * (p >> 3) : 512 + 2 * h + (p - 16);
            for (int e = 0; e < 8; ++e) c.v[o++] = Fval(n, 16 * s + 8 * hh + e); } }
    return c;
}
template <int I> constexpr Chunk mkT1() {
    Chunk c{}; int o = 0;
    for (int b = 6 * I; b < 6 * I + 6; ++b) for (int t = 0; t < 2; ++t) for (int l = 0; l < 64; ++l) for (int e = 0; e < 6; ++e) {
        const int ro = l & 15, a = 4 * e + (l >> 4), n = 24 * a + b;
        c.v[o++] = ro >= 12 ? (u16)0 : (t && !ro) ? base.u[(a & 1) * 288] : base.u[(ro * n + 144 * t) % 576]; }
    return c;
}
constexpr Chunk mkT2() {
    Chunk c{}; int o = 0;
    for (int sp = 0; sp < 2; ++sp) for (int T = 0; T < 3; ++T) for (int t = 0; t < 2; ++t) for (int l = 0; l < 64; ++l) for (int e = 0; e < 6; ++e) {
        const int m = l & 15, d = 8 * T + (m >> 1), po = m & 1, b = 6 * (l >> 4) + 3 * t + (e >> 1), pi = e & 1;
        u16 v = 0;
        if (!sp) v = base.c[(24 * b * d + (po ? (pi ? 0 : 144) + (d >= 12 ? 288 : 0) : (pi ? 432 : 0))) % 576];
        else if (d == 0) v = pi ? (u16)0 : (po && (b & 1)) ? NDCV : DCV;
        else if (d < 12) v = pi ? (u16)0 : base.c[(24 * b * d + 144 * po) % 576];
        else v = pi ? base.c[((12 + 24 * (d - 12)) * b + 144 * po) % 576] : (u16)0;
        c.v[o++] = v; }
    return c;
}
template <int I> constexpr Chunk f2c = mkF2<I>();
struct Table { Chunk c[72]; };
struct Small { Chunk c[5]; };
template <int... I> constexpr Table mkF2T(std::integer_sequence<int, I...>) { return Table{{f2c<I>...}}; }
}
__device__ __attribute__((aligned(256))) const tbl::Table F2H = tbl::mkF2T(std::make_integer_sequence<int, 72>{});
__device__ __attribute__((aligned(256))) const tbl::Small TFF = {{tbl::mkT1<0>(), tbl::mkT1<1>(), tbl::mkT1<2>(), tbl::mkT1<3>(), tbl::mkT2()}};

constexpr size_t CELLS = (size_t)72 * S * 16;
constexpr size_t GIMG = 82944;
constexpr size_t HIMG = 62208;
constexpr size_t KVCH = 18432;
constexpr size_t al(size_t x) { return (x + 4095) & ~(size_t)4095; }
constexpr size_t O_XH0 = 0, O_XH1 = O_XH0 + CELLS, O_XH2 = O_XH1 + CELLS, O_XH3 = O_XH2 + CELLS;
constexpr size_t O_OA = O_XH3 + CELLS, O_OB = O_OA + CELLS, O_X3 = O_OB + CELLS;
constexpr size_t O_WT = O_X3 + CELLS;
constexpr size_t O_WO2 = O_WT + (size_t)256 * 288 * 32;
constexpr size_t O_WOH = O_WO2 + al(8 * GIMG + 4096);
constexpr size_t O_WQH = O_WOH + al(8 * GIMG + 4096);
constexpr size_t O_KVA = O_WQH + al(32 * HIMG + 4096), O_KVB = O_KVA + 256 * KVCH;
constexpr size_t O_SS0 = O_KVB + 256 * KVCH, O_SS2 = O_SS0 + (size_t)S * 16;
constexpr size_t O_FLG = O_SS2 + (size_t)S * 16;
constexpr size_t O_DBG = O_FLG + 131072;

__device__ __forceinline__ unsigned pk(float a, float b) { f32x2 t = {a, b}; return __builtin_bit_cast(unsigned, __builtin_convertvector(t, bf16x2)); }
__device__ __forceinline__ float blo(unsigned u) { return __uint_as_float(u << 16); }
__device__ __forceinline__ float bhi(unsigned u) { return __uint_as_float(u & 0xFFFF0000u); }
__device__ __forceinline__ float sq2(unsigned v, float q) { const bf16x2 t = __builtin_bit_cast(bf16x2, v); return __builtin_amdgcn_fdot2_f32_bf16(t, t, q, false); }
__device__ __forceinline__ float sum2(unsigned v, float q) { return __builtin_amdgcn_fdot2_f32_bf16(__builtin_bit_cast(bf16x2, v), __builtin_bit_cast(bf16x2, 0x3F803F80u), q, false); }
__device__ __forceinline__ rsrc_t mkrsrc(const void* p, int bytes) { return __builtin_amdgcn_make_buffer_rsrc((void*)p, (short)0, bytes, 0x00020000); }
__device__ __forceinline__ bf16x8 asb(u32x4 v) { return __builtin_bit_cast(bf16x8, v); }
__device__ __forceinline__ void glds16(const void* g, unsigned lds_base) {
    unsigned sv;
    asm volatile("s_mov_b32 %0, m0\n\ts_mov_b32 m0, %2\n\ts_nop 0\n\tglobal_load_lds_dwordx4 %1, off\n\ts_mov_b32 m0, %0" : "=&s"(sv) : "v"(g), "s"(lds_base) : "memory");
}
#define WAIT_BAR(N) asm volatile("s_waitcnt vmcnt(" #N ") lgkmcnt(0)\n\ts_barrier" ::: "memory")
#define MF16(a, b, c) __builtin_amdgcn_mfma_f32_16x16x32_bf16(a, b, c, 0, 0, 0)
#define MF32(a, b, c) __builtin_amdgcn_mfma_f32_32x32x16_bf16(a, b, c, 0, 0, 0)
#define SB() __builtin_amdgcn_sched_barrier(0)
__device__ __forceinline__ u32x4 lds128(const char* p) { return *(const LAS u32x4*)(p); }
__device__ __forceinline__ unsigned gimg_off(int kb, int c) {
    const int a = c / 72, x = c - 72 * a, s = kb >> 2, g = kb & 3;
    unsigned o;
    if (x < 64) { const int T = 2 * (x >> 5) + ((x >> 2) & 1), p = 4 * ((x >> 3) & 3) + (x & 3); o = T * 1024 + g * 256 + p * 16; }
    else o = 4096 + g * 128 + (x - 64) * 16;
    return a * (unsigned)GIMG + s * 4608 + o;
}
__device__ __forceinline__ int ofeat(int kb, int e) {
    return kb < 64 ? 18 * (kb >> 1) + (e & 3) + 8 * (e >> 2) + 4 * (kb & 1) : 18 * (4 * (kb - 64) + (e >> 1)) + 16 + (e & 1);
}

__device__ __forceinline__ void store_xh(f32x4 (&acc)[5], char* xh, char* ssp, int a, int r, int g) {
    float q = 0.f;
#pragma unroll
    for (int u = 0; u < 2; ++u) {
        u32x4 o = {pk(acc[2 * u][0], acc[2 * u][1]), pk(acc[2 * u][2], acc[2 * u][3]), pk(acc[2 * u + 1][0], acc[2 * u + 1][1]), pk(acc[2 * u + 1][2], acc[2 * u + 1][3])};
        *(u32x4*)(xh + ((size_t)(9 * a + 4 * u + g) * S + r) * 16) = o;
        if (u == 0 && a == 0 && g == 0) o[0] &= 0xFFFF0000u;
#pragma unroll
        for (int i = 0; i < 4; ++i) q = sq2(o[i], q);
    }
    if (g < 2) {
        u32x2 o = {pk(acc[4][0], acc[4][1]), pk(acc[4][2], acc[4][3])};
        *(u32x2*)(xh + ((size_t)(9 * a + 8) * S + r) * 16 + g * 8) = o;
        q = sq2(o[0], q);
        q = sq2(o[1], q);
    }
    q += __shfl_xor(q, 16); q += __shfl_xor(q, 32);
    if (g == 0) *(u16*)(ssp + r * 16 + a * 2) = (u16)pk(q, 0.f);
}

__device__ __forceinline__ unsigned himg_off(int kb, int j) {
    const int which = j / 576, rem = j - which * 576, h = rem / 18, d = rem - h * 18, s = kb >> 1, hh = kb & 1;
    unsigned o;
    if (which < 2 && d < 16) o = hh * 512 + (16 * which + (d & 3) + 8 * ((d >> 2) & 1) + 4 * (d >> 3)) * 16;
    else o = 1024 + hh * 352 + (which == 2 ? d : 2 + 2 * which + d) * 16;
    return h * (unsigned)HIMG + s * 1728 + o;
}

#define STG(base, slot, i)
template <int MODE>
__device__ __forceinline__ void fft576(const u16* __restrict__ in, char* __restrict__ ws, char* lds, int vb, int w, int lane) {
    const int v = lane & 15, g = lane >> 4, vec = vb * 16 + v;
    u32x4 df[6];
    u32x3 raw[6];
    if (MODE != 1) {
        const rsrc_t rs = mkrsrc(in, MODE ? D * D * 2 : S * D * 2);
        const int off = (MODE ? ofeat(vec >> 3, vec & 7) : vec) * 1152 + 48 * g + 12 * w;
#pragma unroll
        for (int e = 0; e < 6; ++e) raw[e] = __builtin_amdgcn_raw_buffer_load_b96(rs, off + 192 * e, 0, 0);
    } else {
        const u16* src = in + (size_t)(24 * g + 6 * w) * 1728 + vec;
#pragma unroll
        for (int bl = 0; bl < 6; ++bl)
#pragma unroll
            for (int k = 0; k < 3; ++k) df[bl][k] = src[(192 * k + bl) * 1728] | ((unsigned)src[(192 * k + 96 + bl) * 1728] << 16);
    }
    const rsrc_t rt = mkrsrc(&TFF, sizeof(TFF));
    u32x4 t1[12], t2[6], t0[6];
#pragma unroll
    for (int i = 0; i < 12; ++i) { const u32x3 t = __builtin_amdgcn_raw_buffer_load_b96(rt, lane * 12 + i * 768, w * 9216, 0); t1[i] = u32x4{t[0], t[1], t[2], 0u}; }
#pragma unroll
    for (int i = 0; i < 6; ++i) { const u32x3 t = __builtin_amdgcn_raw_buffer_load_b96(rt, lane * 12 + i * 768, 36864, 0); t2[i] = u32x4{t[0], t[1], t[2], 0u}; }
    const rsrc_t rz = mkrsrc(&TFF, w ? 0 : (int)sizeof(TFF));
#pragma unroll
    for (int i = 0; i < 6; ++i) { const u32x3 z = __builtin_amdgcn_raw_buffer_load_b96(rz, lane * 12 + i * 768, 41472, 0); t0[i] = u32x4{z[0], z[1], z[2], 0u}; }
    SB();
    if (MODE != 1) {
#pragma unroll
        for (int bp = 0; bp < 3; ++bp)
#pragma unroll
            for (int k = 0; k < 3; ++k) {
                df[2 * bp][k] = __builtin_amdgcn_perm(raw[2 * k + 1][bp], raw[2 * k][bp], 0x05040100u);
                df[2 * bp + 1][k] = __builtin_amdgcn_perm(raw[2 * k + 1][bp], raw[2 * k][bp], 0x07060302u);
            }
    }
    unsigned y[4][6];
#pragma unroll
    for (int bl = 0; bl < 6; ++bl) {
        df[bl][3] = 0u;
        const f32x4 re = MF16(asb(t1[2 * bl]), asb(df[bl]), f32x4{}), im = MF16(asb(t1[2 * bl + 1]), asb(df[bl]), f32x4{});
#pragma unroll
        for (int r = 0; r < 4; ++r) y[r][bl] = pk(re[r], im[r]);
    }
    if (g < 3) {
#pragma unroll
        for (int r = 0; r < 4; ++r)
#pragma unroll
            for (int t = 0; t < 2; ++t) *(LAS u32x4*)(lds + (4 * g + r) * 2048 + t * 1024 + w * 256 + v * 16) = u32x4{y[r][3 * t], y[r][3 * t + 1], y[r][3 * t + 2], 0u};
    }
    asm volatile("s_waitcnt lgkmcnt(0)\n\ts_barrier" ::: "memory");
    f32x4 acc[9];
    u32x4 y0[3], y1[3];
#pragma unroll
    for (int sl = 0; sl < 3; ++sl) { y0[sl] = lds128(lds + (3 * w + sl) * 2048 + lane * 16); y1[sl] = lds128(lds + (3 * w + sl) * 2048 + 1024 + lane * 16); }
    auto stage2 = [&](int sl, const u32x4 (&m)[6]) {
#pragma unroll
        for (int T = 0; T < 3; ++T) {
            if (MODE != 2) acc[3 * sl + T] = MF16(asb(m[2 * T + 1]), asb(y1[sl]), MF16(asb(m[2 * T]), asb(y0[sl]), f32x4{}));
            else acc[3 * sl + T] = MF16(asb(y1[sl]), asb(m[2 * T + 1]), MF16(asb(y0[sl]), asb(m[2 * T]), f32x4{}));
        }
    };
    if (w == 0) { stage2(0, t0); asm volatile("" ::: "memory"); } else stage2(0, t2);
    stage2(1, t2); stage2(2, t2);
    u32x2 o[9];
#pragma unroll
    for (int i = 0; i < 9; ++i) o[i] = u32x2{pk(acc[i][0], acc[i][1]), pk(acc[i][2], acc[i][3])};
    if (MODE == 0) {
        char* xh = ws + O_XH0 + (size_t)vec * 16;
#pragma unroll
        for (int u = 0; u < 4; ++u) *(u32x4*)(xh + (size_t)(16 * w + 4 * u + g) * (S * 16)) = u32x4{o[2 * u][0], o[2 * u][1], o[2 * u + 1][0], o[2 * u + 1][1]};
        *(u32x2*)(xh + (size_t)(64 + 2 * w + (g >> 1)) * (S * 16) + (g & 1) * 8) = o[8];
        if (w == 0 && g == 0) o[0][0] &= 0xFFFF0000u;
        float q = 0.f;
#pragma unroll
        for (int i = 0; i < 9; ++i) { q = sq2(o[i][0], q); q = sq2(o[i][1], q); }
        q += __shfl_xor(q, 16); q += __shfl_xor(q, 32);
        if (g == 0) *(unsigned*)(ws + O_SS0 + vec * 16 + w * 4) = pk(q, 0.f);
    } else if (MODE == 1) {
        if (w == 0 && g == 0) o[0][0] &= 0xFFFF0000u;
        char* d = ws + O_WQH + himg_off(16 * w + g, vec);
#pragma unroll
        for (int u = 0; u < 4; ++u) *(u32x4*)(d + u * 3456) = u32x4{o[2 * u][0], o[2 * u][1], o[2 * u + 1][0], o[2 * u + 1][1]};
        *(u32x2*)(ws + O_WQH + himg_off(64 + 2 * w + (g >> 1), vec) + (g & 1) * 8) = o[8];
    } else {
#pragma unroll
        for (int i = 0; i < 9; ++i) {
            const int c = i < 8 ? 8 * (16 * w + 4 * (i >> 1) + (v >> 2)) + 4 * (i & 1) + (v & 3) : 512 + 16 * w + v;
            *(u32x2*)(ws + O_WOH + gimg_off(2 * vb + (g >> 1), c) + (g & 1) * 8) = o[i];
        }
    }
}
__device__ __forceinline__ void touch(const char* p, int bytes, int t, int nt) {
    unsigned d = 0;
    for (int o = t * 128; o < bytes; o += nt * 128) asm volatile("global_load_dword %0, %1, off" : "+v"(d) : "v"(p + o) : "memory");
    asm volatile("s_waitcnt vmcnt(0)" : "+v"(d));
}

constexpr int B_X = 8, B_WT = B_X + 128, B_WO = B_WT + 72, B_PF = B_WO + 40, B_END = B_PF + 48;
__global__ __launch_bounds__(256, 2) void k_in(const u16* __restrict__ x, const u16* __restrict__ wr, const u16* __restrict__ wi,
                                            const u16* __restrict__ wqkv, const u16* __restrict__ wo, char* __restrict__ ws) {
    __shared__ __attribute__((aligned(16))) char lds[24576];
    const char* pc; int len;
    asm volatile("s_getpc_b64 %0\n1:\n s_mov_b32 %1, text_end_mark - 1b" : "=s"(pc), "=s"(len));
    const int tid = threadIdx.x, lane = tid & 63, w = __builtin_amdgcn_readfirstlane(tid >> 6), bx = blockIdx.x;
    STG(ws + O_DBG + 2 * 262144, bx * 4 + w, 0);
    if (__builtin_expect(bx < B_X, 1)) {
        touch(pc, len, tid, 256);
        *(unsigned*)(ws + O_FLG + (bx * 128 + (tid & 127)) * 128) = 0u;
    } else if (bx < B_WT) fft576<0>(x, ws, lds, bx - B_X, w, lane);
    else if (bx < B_WO) {
        const int L = ((bx - B_WT) * 4 + w) * 16 + (lane >> 2), tq = L / 72, cq = L - tq * 72, t = 4 * tq + (lane & 3);
        const int k0 = ((16 * (t >> 6) + (t & 15) + 256 * ((t >> 4) & 3)) * 289 + 4 * cq + 1) * 2;
        const rsrc_t rr = mkrsrc(wr, S * 289 * 2), ri = mkrsrc(wi, S * 289 * 2);
        u32x2 a[8], c[8];
#pragma unroll
        for (int i = 0; i < 8; ++i) {
            a[i] = __builtin_amdgcn_raw_buffer_load_b64(rr, k0, 128 * 289 * (16 * (i >> 2) + (i & 3)), 0);
            c[i] = __builtin_amdgcn_raw_buffer_load_b64(ri, k0, 128 * 289 * (16 * (i >> 2) + (i & 3)), 0);
        }
#pragma unroll
        for (int u = 0; u < 4; ++u) {
            const int j1 = 4 * cq + 1 + u, jj = j1 == 288 ? 0 : j1, q = jj / 24, m = jj - 24 * q;
            const int sg = m > 12 ? 24 - m : m == 12 ? 0 : m, dd = m > 12 ? 23 - q : m == 12 ? 12 + q : q;
            const int ww = sg / 3, tau = 3 * (sg - 3 * ww) + (dd >> 3), gg = (dd >> 1) & 3;
            const int ch = tau < 8 ? 4 * (16 * ww + 4 * (tau >> 1) + gg) + 2 * (tau & 1) + (dd & 1) : 256 + 8 * ww + 2 * gg + (dd & 1);
            unsigned o[8];
#pragma unroll
            for (int i = 0; i < 8; ++i) o[i] = __builtin_amdgcn_perm(c[i][u >> 1], a[i][u >> 1], (u & 1) ? 0x07060302u : 0x05040100u);
            char* d = ws + O_WT + ch * 8192 + t * 16;
            *(u32x4*)d = u32x4{o[0], o[1], o[2], o[3]}; *(u32x4*)(d + 4096) = u32x4{o[4], o[5], o[6], o[7]};
        }
    } else if (bx < B_PF) { if (bx - B_WO < 36) fft576<2>(wo, ws, lds, bx - B_WO, w, lane); }
    else {
        unsigned d = 0;
        for (int i = ((bx - B_PF) >> 3) * 256 + tid; i < 2880; i += 1536) {
            const int row = i / 5, col = (bx & 7) * 448 + (i - 5 * row) * 111;
            if (col < 3456) asm volatile("global_load_ubyte %0, %1, off" : "+v"(d) : "v"((const char*)wqkv + row * 3456 + col) : "memory");
        }
        asm volatile("s_waitcnt vmcnt(0)" : "+v"(d));
    }
    STG(ws + O_DBG + 2 * 262144, bx * 4 + w, 3);
}

__device__ __forceinline__ u32x4 neg8(u32x4 v) { return v ^ 0x80008000u; }
__global__ __launch_bounds__(512) void k_fft(const char* __restrict__ xin, char* __restrict__ xout, const char* __restrict__ ssp, char* __restrict__ ws,
                                             const u16* __restrict__ wqkv, const u16* __restrict__ wo) {
    __shared__ __attribute__((aligned(16))) char lds[32768];
    const int tid = threadIdx.x, lane = tid & 63, w8 = __builtin_amdgcn_readfirstlane(tid >> 6), w = w8 & 3, z = w8 >> 2, col = lane & 15, g = lane >> 4;
#define STF(i) STG(ws + O_DBG + (wqkv ? 3 : 4) * 262144, blockIdx.x * 8 + w8, i)
    STF(0);
    if (blockIdx.x >= 144) {
        const int e = blockIdx.x - 144, xcd = e & 7, j = e >> 3;
        if (wqkv) {
            if (z == 0 && xcd * 14 + j < 108) fft576<1>(wqkv, ws, lds, xcd * 14 + j, w, lane);
        } else if (e < 41) {
            u32x4 o[2]; int kb[2], n[2];
#pragma unroll
            for (int u = 0; u < 2; ++u) {
                const int c1 = e * 1024 + u * 512 + tid, co = c1 < 41472 ? c1 : 41471; kb[u] = co / 576; n[u] = co - kb[u] * 576;
#pragma unroll
                for (int i = 0; i < 4; ++i) o[u][i] = wo[ofeat(kb[u], 2 * i) * D + n[u]] | ((unsigned)wo[ofeat(kb[u], 2 * i + 1) * D + n[u]] << 16);
            }
#pragma unroll
            for (int u = 0; u < 2; ++u) *(u32x4*)(ws + O_WO2 + gimg_off(kb[u], n[u])) = o[u];
        } else if (j >= 6) {
            const int h = (xcd & 3) * 8 + j - 6;
            touch((const char*)&F2H + h * 20736, 20736, tid, 512);
            touch(ws + O_WQH + h * HIMG, HIMG, tid, 512);
        }
        STF(3);
        return;
    }
    const int bx = blockIdx.x, kb = (bx & 7) * 9 + (bx >> 4), half = (bx >> 3) & 1;
    const bool dc = bx == 0;
    const int n0 = 32 * (16 * w + 4 * g) + 16 * z + col;
    const size_t cell0 = ((size_t)kb * S + n0) * 16 + half * 8;
    u32x2 xo[4]; u32x4 ss[4];
#pragma unroll
    for (int i = 0; i < 4; ++i) { xo[i] = *(const u32x2*)(xin + cell0 + i * 512); ss[i] = *(const u32x4*)(ssp + n0 * 16 + i * 512); }
    const char* wp = ws + O_WT + (4 * kb + 2 * half + z) * 8192 + (tid & 255) * 16;
    const u32x4 w0 = *(const u32x4*)wp, w1 = *(const u32x4*)(wp + 4096);
    u32x4 f64r[2], f64i[2], f32r[2], f32i[2];
    float t1c[8], t1s[8], t2c[8], t2s[8];
    {
        const float k1 = (float)(16 * w + col), fc = (float)col;
        float c[4], s[4];
#pragma unroll
        for (int i = 0; i < 4; ++i) { const float a = (float)(4 * g + i) * k1 * (1.f / 64.f); c[i] = __builtin_amdgcn_cosf(a); s[i] = -__builtin_amdgcn_sinf(a); }
        const unsigned odd = (col & 1) ? 0x80008000u : 0u;
        {
            const unsigned r0 = pk(c[0], c[1]), r1 = pk(c[2], c[3]), i0 = pk(s[0], s[1]), i1 = pk(s[2], s[3]);
            const unsigned sr = (col & 2) ? 0x80008000u : 0u, si = sr ^ odd;
            f64r[0] = u32x4{r0, r1, ((col & 1) ? i0 : r0) ^ sr, ((col & 1) ? i1 : r1) ^ sr};
            f64i[0] = u32x4{i0, i1, ((col & 1) ? r0 : i0) ^ si, ((col & 1) ? r1 : i1) ^ si};
            f64r[1] = f64r[0] ^ odd; f64i[1] = f64i[0] ^ odd;
        }
#pragma unroll
        for (int i = 0; i < 4; ++i) { const float a = (float)(4 * g + i) * fc * (1.f / 32.f); c[i] = __builtin_amdgcn_cosf(a); s[i] = -__builtin_amdgcn_sinf(a); }
        {
            const unsigned r0 = pk(c[0], c[1]), r1 = pk(c[2], c[3]), i0 = pk(s[0], s[1]), i1 = pk(s[2], s[3]);
            f32r[0] = u32x4{r0, r1, r0 ^ odd, r1 ^ odd}; f32i[0] = u32x4{i0, i1, i0 ^ odd, i1 ^ odd};
            f32r[1] = f32r[0] ^ 0x80000000u; f32i[1] = f32i[0] ^ 0x80000000u;
        }
#pragma unroll
        for (int i = 0; i < 8; ++i) {
            const float a = (float)(16 * (i >> 2) + 4 * g + (i & 3)) * k1 * (1.f / 2048.f);
            t1c[i] = __builtin_amdgcn_cosf(a); t1s[i] = __builtin_amdgcn_sinf(a);
            const float b = (float)(16 * (i >> 2) + col) * (float)(16 * w + 4 * g + (i & 3)) * (1.f / 2048.f);
            t2c[i] = __builtin_amdgcn_cosf(b); t2s[i] = __builtin_amdgcn_sinf(b);
        }
    }
    u32x4 n64i[2] = {neg8(f64i[0]), neg8(f64i[1])}, n32i[2] = {neg8(f32i[0]), neg8(f32i[1])};
#pragma unroll
    for (int i = 0; i < 2; ++i) asm volatile("" : "+v"(f64r[i]), "+v"(f64i[i]), "+v"(n64i[i]), "+v"(f32r[i]), "+v"(f32i[i]), "+v"(n32i[i]));
#pragma unroll
    for (int i = 0; i < 8; ++i) asm volatile("" : "+v"(t1c[i]), "+v"(t1s[i]), "+v"(t2c[i]), "+v"(t2s[i]));
    SB();
    char* xw = lds + (w >> 1) * 4096 + lane * 16 + (w & 1) * 8;
    f32x4 zr[2], zi[2];
    {
        float rho[4];
#pragma unroll
        for (int i = 0; i < 4; ++i) {
            float q = 0.f;
#pragma unroll
            for (int k = 0; k < 4; ++k) q = sum2(ss[i][k], q);
            rho[i] = __builtin_amdgcn_rsqf(q * (4194304.f / 576.f) + 41.94304f);
        }
#pragma unroll
        for (int c = 0; c < 2; ++c) {
#pragma unroll
            for (int i = 0; i < 4; ++i) { zr[c][i] = blo(xo[i][c]); zi[c][i] = bhi(xo[i][c]); }
            if (c == 0 && dc) { const f32x4 t = zr[0]; zr[0] = zi[0]; zi[0] = t; }
            const u32x2 re = {pk(zr[c][0] * rho[0], zr[c][1] * rho[1]), pk(zr[c][2] * rho[2], zr[c][3] * rho[3])};
            u32x2 im = {pk(zi[c][0] * rho[0], zi[c][1] * rho[1]), pk(zi[c][2] * rho[2], zi[c][3] * rho[3])};
            if (c == 0 && dc) im = u32x2{0u, 0u};
            *(LAS u32x2*)(xw + c * 8192 + z * 2048) = re; *(LAS u32x2*)(xw + c * 8192 + z * 2048 + 1024) = im;
        }
    }
    float wc[8], wd[8];
#pragma unroll
    for (int i = 0; i < 8; ++i) { const unsigned wv = i < 4 ? w0[i & 3] : w1[i & 3]; wc[i] = blo(wv); wd[i] = bhi(wv); asm volatile("" : "+v"(wc[i]), "+v"(wd[i])); }
    __syncthreads();
    const char* xr = lds + lane * 16;
    f32x4 yr[2] = {}, yi[2] = {};
    u32x4 ar[4], ai[4];
#pragma unroll
    for (int q = 0; q < 4; ++q) { ar[q] = lds128(xr + z * 8192 + (q & 1) * 4096 + (q >> 1) * 2048); ai[q] = lds128(xr + z * 8192 + (q & 1) * 4096 + (q >> 1) * 2048 + 1024); }
    SB();
#pragma unroll
    for (int t = 0; t < 2; ++t)
#pragma unroll
        for (int s = 0; s < 2; ++s) {
            yr[t] = MF16(asb(ar[2 * t + s]), asb(f64r[s]), yr[t]); yi[t] = MF16(asb(ar[2 * t + s]), asb(f64i[s]), yi[t]);
            yr[t] = MF16(asb(ai[2 * t + s]), asb(n64i[s]), yr[t]); yi[t] = MF16(asb(ai[2 * t + s]), asb(f64r[s]), yi[t]);
        }
    u32x4 br, bi;
    {
        float vr[8], vi[8];
#pragma unroll
        for (int i = 0; i < 8; ++i) { const float a = yr[i >> 2][i & 3], b = yi[i >> 2][i & 3]; vr[i] = a * t1c[i] + b * t1s[i]; vi[i] = b * t1c[i] - a * t1s[i]; }
#pragma unroll
        for (int q = 0; q < 4; ++q) { br[q] = pk(vr[2 * q], vr[2 * q + 1]); bi[q] = pk(vi[2 * q], vi[2 * q + 1]); }
    }
#pragma unroll
    for (int t = 0; t < 2; ++t) {
        yr[t] = MF16(asb(f32r[t]), asb(br), f32x4{}); yi[t] = MF16(asb(f32i[t]), asb(br), f32x4{});
        yr[t] = MF16(asb(n32i[t]), asb(bi), yr[t]); yi[t] = MF16(asb(f32r[t]), asb(bi), yi[t]);
    }
    {
        float vr[8], vi[8];
#pragma unroll
        for (int i = 0; i < 8; ++i) { const float a = yr[i >> 2][i & 3], b = yi[i >> 2][i & 3]; vr[i] = a * wc[i] - b * wd[i]; vi[i] = a * wd[i] + b * wc[i]; }
#pragma unroll
        for (int q = 0; q < 4; ++q) { br[q] = pk(vr[2 * q], vr[2 * q + 1]); bi[q] = pk(vi[2 * q], vi[2 * q + 1]); }
    }
#pragma unroll
    for (int t = 0; t < 2; ++t) {
        yr[t] = MF16(asb(br), asb(f32r[t]), f32x4{}); yi[t] = MF16(asb(br), asb(n32i[t]), f32x4{});
        yr[t] = MF16(asb(bi), asb(f32i[t]), yr[t]); yi[t] = MF16(asb(bi), asb(f32r[t]), yi[t]);
    }
    {
        float vr[8], vi[8];
#pragma unroll
        for (int i = 0; i < 8; ++i) { const float a = yr[i >> 2][i & 3], b = yi[i >> 2][i & 3]; vr[i] = a * t2c[i] - b * t2s[i]; vi[i] = b * t2c[i] + a * t2s[i]; }
#pragma unroll
        for (int t = 0; t < 2; ++t) {
            *(LAS u32x2*)(xw + 16384 + z * 8192 + t * 2048) = u32x2{pk(vr[4 * t], vr[4 * t + 1]), pk(vr[4 * t + 2], vr[4 * t + 3])};
            *(LAS u32x2*)(xw + 16384 + z * 8192 + t * 2048 + 1024) = u32x2{pk(vi[4 * t], vi[4 * t + 1]), pk(vi[4 * t + 2], vi[4 * t + 3])};
        }
    }
    __syncthreads();
#pragma unroll
    for (int q = 0; q < 4; ++q) { ar[q] = lds128(xr + 16384 + q * 4096 + z * 2048); ai[q] = lds128(xr + 16384 + q * 4096 + z * 2048 + 1024); }
    SB();
    const f32x4 keep = zi[0];
#pragma unroll
    for (int c = 0; c < 2; ++c)
#pragma unroll
        for (int s = 0; s < 2; ++s) {
            zr[c] = MF16(asb(f64r[s]), asb(ar[2 * c + s]), zr[c]); zi[c] = MF16(asb(n64i[s]), asb(ar[2 * c + s]), zi[c]);
            zr[c] = MF16(asb(f64i[s]), asb(ai[2 * c + s]), zr[c]); zi[c] = MF16(asb(f64r[s]), asb(ai[2 * c + s]), zi[c]);
        }
    if (dc) { zi[0] = zr[0]; zr[0] = keep; }
#pragma unroll
    for (int i = 0; i < 4; ++i) *(u32x2*)(xout + cell0 + i * 512) = u32x2{pk(zr[0][i], zi[0][i]), pk(zr[1][i], zi[1][i])};
    STF(3);
}

constexpr float QSCALE = 0.23570226039551584f * 1.4426950408889634f;
#define EX(x) __builtin_amdgcn_exp2f(x)
__device__ __forceinline__ float xhalf_max(float m) {
    auto rr = __builtin_amdgcn_permlane32_swap(__float_as_uint(m), __float_as_uint(m), false, false);
    return fmaxf(__uint_as_float(rr[0]), __uint_as_float(rr[1]));
}
__device__ __forceinline__ float max16(const f32x16& s) {
    float a = fmaxf(fmaxf(s[0], s[1]), s[2]), b = fmaxf(fmaxf(s[3], s[4]), s[5]);
    a = fmaxf(fmaxf(a, s[6]), s[7]); b = fmaxf(fmaxf(b, s[8]), s[9]); a = fmaxf(fmaxf(a, s[10]), s[11]); b = fmaxf(fmaxf(b, s[12]), s[13]);
    a = fmaxf(fmaxf(a, s[14]), s[15]);
    return xhalf_max(fmaxf(a, b));
}
constexpr int ATT_CONST = 8 * (int)KVCH;
constexpr int L_W = 3 * (int)KVCH, L_F2 = L_W + 65536, L_SC = (int)KVCH, L_CNT = ATT_CONST + 10240;
#define ST(i)
#define STP(n)
#define A_XP(S, D) "v_exp_f32 v[" D "], v[" S "]\n v_exp_f32 v[" D "+1], v[" S "+1]\n v_exp_f32 v[" D "+2], v[" S "+2]\n v_exp_f32 v[" D "+3], v[" S "+3]\n"
#define A_QK1(SN, KC) "v_mfma_f32_32x32x16_bf16 v[" SN ":" SN "+15], v[" KC ":" KC "+3], v[76:79], 0\n"
#define A_QK2(SN, K2, QN) "v_mfma_f32_32x32x8bf16_1k v[" SN ":" SN "+15], v[" K2 ":" K2 "+1], v[" QN ":" QN "+1], v[" SN ":" SN "+15]\n"
#define A_PV(V, P) "v_smfmac_f32_32x32x32_bf16 v[0:15], v[" V ":" V "+3], v[" P ":" P "+7], v84\n"
#define A_R128(D, A, O) "ds_read_b128 v[" D ":" D "+3], " A " offset:" O "\n"
#define A_R32(D, O) "ds_read_b32 v" D ", v86 offset:" O "\n"
#define A_STEP(SC, Q1, Q2, LD) Q1 A_XP(SC, SC) Q2 A_XP(SC "+4", SC "+4") "s_waitcnt lgkmcnt(0)\n" A_PV("72", "48") A_XP(SC "+8", "48") A_PV("68", SC) LD A_XP(SC "+12", "52")
#define A_VV(T) A_R128("72", "v87", #T "*1152+576") A_R128("68", "v87", #T "*1152+1152")
#define A_PR(N) "s_setprio " #N "\n"
#define A_S0(H) A_STEP("16", A_QK1("32", "60"), A_QK2("32", "64", "82"), A_VV(0) A_R128("60", "v85", "3072") H)
#define A_S1(H) A_STEP("32", A_QK1("16", "56"), A_QK2("16", "66", "80"), A_VV(1) A_R128("56", "v85", "4096") A_R32("64", "512") H)
#define A_S2(H) A_STEP("16", A_QK1("32", "60"), A_QK2("32", "66", "82"), A_VV(2) A_R128("60", "v85", "5120") H)
#define A_S3(H) A_STEP("32", A_QK1("16", "56"), A_QK2("16", "64", "80"), A_VV(3) A_R128("56", "v85", "6144") A_R32("66", "768") H)
#define A_S4(H) A_STEP("16", A_QK1("32", "60"), A_QK2("32", "64", "82"), A_VV(4) A_R128("60", "v85", "7168") H)
#define A_S5(H) A_STEP("32", A_QK1("16", "56"), A_QK2("16", "66", "80"), A_VV(5) H)
#define A_S6(H) A_STEP("16", A_QK1("32", "60"), A_QK2("32", "66", "82"), A_VV(6) H)
#define A_NEXT "v_add_u32 v87, v87, v88\n v_add_u32 v85, 0x4800, v85\n v_add_u32 v86, 0x4800, v86\n"
#define A_S567 A_S5(A_R128("56", "v85", "18432") A_R32("64", "18432")) A_S6(A_R128("60", "v85", "19456")) \
    A_STEP("32", A_QK1("16", "56"), A_QK2("16", "64", "80"), A_R128("72", "v87", "8640") A_NEXT A_R128("68", "v87", "0") A_R128("56", "v85", "2048") A_R32("66", "256"))
#define A_PRIF(N, P, L) "s_cmp_eq_u32 %[n], " #N "\n s_cbranch_scc0 " L "f\n s_setprio " #P "\n" L ":\n"
#define A_D1 "s_add_u32 m0, %[dm], %[lb]\n s_nop 0\n buffer_load_dwordx4 v89, %[rw], %[dm] offen lds\n s_add_u32 %[dm], %[dm], 0x2000\n"
#define A_DMA(L, X) "s_cmp_eq_u32 %[n], 2\n s_cbranch_scc0 " L "f\n" A_D1 A_D1 X L ":\n"
#define A_S567P A_S5(A_R128("56", "v85", "18432") A_R32("64", "18432")) A_PRIF(1, 0, "53") A_PRIF(3, 0, "56") A_S6(A_R128("60", "v85", "19456")) \
    A_STEP("32", A_QK1("16", "56"), A_QK2("16", "64", "80"), A_R128("72", "v87", "8640") A_NEXT A_R128("68", "v87", "0") A_R128("56", "v85", "2048") A_R32("66", "256"))
#define A_PRIME A_R128("56", "v85", "0") A_R32("64", "0") A_R128("60", "v85", "1024") A_R32("66", "256") A_R128("68", "v87", "0") \
    "s_waitcnt lgkmcnt(3)\n" A_QK1("16", "56") A_QK2("16", "64", "80") A_R128("56", "v85", "2048") "s_waitcnt lgkmcnt(1)\n s_nop 10\n"
#define A_PRIME0 A_R128("56", "v85", "0") A_R32("64", "0") A_R128("60", "v85", "1024") A_R32("66", "256") A_R128("68", "v87", "0") \
    "v_mov_b32 v72, 0\n v_mov_b32 v73, 0\n v_mov_b32 v65, 0x3f80\n v_mov_b32 v67, 0x3f80\n v_mov_b32 v84, 0xdddddddd\n v_subrev_u32 v89, %[lb], v85\n" \
    "v_mfma_f32_32x32x8bf16_1k v[0:15], v[72:73], v[72:73], 0\n" \
    "s_waitcnt lgkmcnt(3)\n" A_QK1("16", "56") A_QK2("16", "64", "80") A_R128("56", "v85", "2048") "s_waitcnt lgkmcnt(1)\n s_nop 10\n"
#define A_S0F A_QK1("32", "60") A_XP("16", "16") A_QK2("32", "64", "82") A_XP("16+4", "16+4") A_XP("16+8", "48") "s_waitcnt lgkmcnt(0)\n" A_PV("68", "16") \
    A_VV(0) A_R128("60", "v85", "3072") A_XP("16+12", "52")
#define A_SIGNAL(L) "s_waitcnt vmcnt(0)\n s_mov_b64 exec, 1\n v_mov_b32 v93, %[cnt]\n v_mov_b32 v94, 1\n v_mov_b32 v95, %[mine]\n ds_add_rtn_u32 v92, v93, v94\n" \
    "s_waitcnt lgkmcnt(0)\n v_readfirstlane_b32 %[tmp], v92\n s_nop 3\n s_cmp_lg_u32 %[tmp], 7\n s_cbranch_scc1 " L "f\n global_store_dword v95, v94, %[fb] sc1\n" \
    L ":\n s_mov_b64 exec, -1\n"
#define A_POLL(L, TEST) "s_mov_b32 %[tmp], 0x200000\n" L ":\n global_load_dword v112, v90, %[fb] sc1\n s_waitcnt vmcnt(0)\n v_cmp_eq_u32 vcc, 1, v112\n s_nop 3\n" \
    TEST "s_cbranch_scc1 " L "0f\n s_sleep 1\n s_sub_u32 %[tmp], %[tmp], 1\n s_cbranch_scc0 " L "b\n" L "0:\n"
#define A_GL(M) "buffer_load_dwordx4 v[92+4*" #M ":95+4*" #M "], v89, %[rk], %[so] offen sc1\n s_add_u32 %[so], %[so], 0x2000\n s_cmp_ge_u32 %[so], 0x24000\n" \
    "s_cselect_b32 %[tmp], 0x24000, 0\n s_sub_u32 %[so], %[so], %[tmp]\n"
#define A_WR(M, A, O) "ds_write_b128 " A ", v[92+4*" #M ":95+4*" #M "] offset:" O "\n"
#define A_BAR "s_waitcnt lgkmcnt(0)\n s_barrier\n"

#define QL0 2
#define QL1 4
#define QLA 8
constexpr int q_hi(int s) {
    int h = QL1 + 1 + 2 * s; if (h > s + QLA) h = s + QLA;
    return h > 35 ? 35 : h;
}
template <bool LAST> constexpr int q_wait(int s) {
    const int hi = q_hi(s); int n = hi - s;
    for (int q = 1; q < 4; ++q) if (9 * q - 1 > s && 9 * q - 1 <= hi) n += (LAST && q < 3) ? 3 : 2;
    return n;
}
template <bool LAST>
__device__ __forceinline__ void qa_body(char* ws, char* out) {
    __shared__ __attribute__((aligned(256))) char lds[L_CNT + 16];
    const int tid = threadIdx.x, lane = tid & 63, w = __builtin_amdgcn_readfirstlane(tid >> 6), r = lane & 31, hh = lane >> 5;
    const int bx = blockIdx.x, b = (bx & 4) + ((bx >> 3) & 3), h = (bx & 3) * 8 + (bx >> 5);
    const char* xh = ws + (LAST ? O_XH3 : O_XH1);
    const int row = b * 256 + w * 32 + r;
    const unsigned lb = (unsigned)(uintptr_t)lds;
    ST(0);
    u32x4 xf[36];
    const char* wsrc = ws + O_WQH + (size_t)h * HIMG + w * 1024 + lane * 16;
    const char* f2 = (const char*)&F2H + (size_t)h * 20736;
    const rsrc_t rx = mkrsrc(xh, (int)CELLS);
    const int xo = (hh * S + row) * 16;
    auto issue = [&](int s) {
        if (s == 0 || s == 8 || s == 17 || s == 26) {
            const int q = (s + 1) / 9;
            glds16(wsrc + (2 * q) * 8192, lb + L_W + w * 1024 + (2 * q) * 8192);
            glds16(wsrc + (2 * q + 1) * 8192, lb + L_W + w * 1024 + (2 * q + 1) * 8192);
            if (LAST && q < 3) { const int of = (q * 8 + w) * 1024 + lane * 16; glds16(f2 + (of < 20736 ? of : 0), lb + L_F2 + w * 1024 + q * 8192); }
        }
        asm volatile("buffer_load_dwordx4 %0, %1, %2, %3 offen" : "=v"(xf[s]) : "v"(xo), "s"(rx), "s"(s * (2 * S * 16)));
    };
#pragma unroll
    for (int s = 0; s < QL0; ++s) issue(s);
    SB();
    if (tid < 128) { const unsigned v = (tid & 1) ? 0u : 0x3F803F80u; *(LAS u32x4*)(lds + ATT_CONST + (tid >> 2) * 288 + (2 + (tid & 3)) * 16) = u32x4{v, v, v, v}; }
    if (tid == 128) *(LAS unsigned*)(lds + L_CNT) = 0u;
    rsrc_t rk = mkrsrc(ws + (LAST ? O_KVB : O_KVA) + (size_t)h * 8 * KVCH, 8 * (int)KVCH);
    rsrc_t rw = mkrsrc(ws + (LAST ? O_WO2 : O_WOH) + (h & 7) * GIMG, (int)GIMG);
    rsrc_t ro = mkrsrc(ws + (LAST ? O_OB : O_OA), (int)CELLS), r3 = mkrsrc(ws + O_X3, (int)CELLS);
    const char* fb = ws + O_FLG + (LAST ? 65536 : 0) + h * 1024;
    const int rp = (r & 19) | ((r & 4) << 1) | ((r & 8) >> 1);
    const bool isv = r < 18;
    const int cslot = ((r >= 20 && r < 28) ? 4 : 2) + ((r == 18 || r == 20) ? 0 : 1);
    int own = b * (int)KVCH, so = (((b + 1) & 7) * (int)KVCH) + w * 1024;
    unsigned ko = w * 1024 + hh * 512 + rp * 16, vo = 9216 + w * 1152 + hh * 288 + r * 16, k2o = 8192 + (w * 32 + rp) * 4;
    unsigned scw = L_SC + w * 640 + r * 4, scl = L_SC + w * 640 + (r - 17) * 128 + 16 * hh, scr = L_SC + w * 640 + 16 * hh;
    unsigned vp = lb + (isv ? 9216 + hh * 288 + r * 16 : ATT_CONST + hh * 288 + cslot * 16), vstep = isv ? (int)KVCH : 0;
    unsigned kp = lb + lane * 16, k2p = lb + 8192 + lane * 4, foff = ((b + 1 + lane) & 7) * 128, pd = lb + (int)KVCH + w * 1024 + lane * 16;
    unsigned oo = ((2 * h + hh) * S + row) * 16, oo2 = ((64 + (h >> 2)) * S + row) * 16 + (h & 3) * 4;
    int mine = b * 128;
    asm volatile("" : "+s"(rk), "+s"(rw), "+s"(fb), "+s"(own), "+s"(so), "+s"(mine));
    asm volatile("" : "+v"(ko), "+v"(vo), "+v"(k2o), "+v"(scw), "+v"(scl), "+v"(scr), "+v"(vp), "+v"(vstep),
                 "+v"(kp), "+v"(k2p), "+v"(foff), "+v"(pd), "+v"(oo), "+v"(oo2));
    f32x16 aa = {}, ab = {}, ac = {};
    float q0 = 0.f, q1 = 0.f;
    const char* pa = lds + L_W + lane * 16;
    const char* pb = lds + L_W + 1024 + hh * 352 + (r < 22 ? r : 21) * 16;
    const char* pc = lds + L_F2 + hh * 288 + (r < 18 ? r : 17) * 16;
    SB();
#pragma unroll
    for (int s = QL0; s <= q_hi(-1); ++s) issue(s);
    asm volatile("s_waitcnt vmcnt(%0)\n\ts_barrier" :: "n"(q_wait<LAST>(-1)) : "memory");
    ST(1);
    u32x4 wa = lds128(pa), wb = lds128(pb), wc = {};
    if (LAST) wc = lds128(pc);
#pragma unroll
    for (int s = 0; s < 36; ++s) {
#pragma unroll
        for (int t = q_hi(s - 1) + 1; t <= q_hi(s); ++t) issue(t);
        if (s == 8 || s == 17 || s == 26) asm volatile("s_waitcnt vmcnt(%1)\n\ts_barrier" : "+v"(xf[s]) : "n"(q_wait<LAST>(s)) : "memory");
        else asm volatile("s_waitcnt vmcnt(%1)" : "+v"(xf[s]) : "n"(q_wait<LAST>(s)));
        u32x4 na = wa, nb = wb, nc = wc;
        if (s < 35) { na = lds128(pa + (s + 1) * 1728); nb = lds128(pb + (s + 1) * 1728); if (LAST) nc = lds128(pc + (s + 1) * 576); }
#define EXP1 0
        if (!(EXP1 & 2) || s == 35) aa = MF32(asb(wa), asb(xf[s]), aa);
        if (!(EXP1 & 1) || s == 35) { q0 = sq2((s == 0 && hh == 0) ? xf[s][0] & 0xFFFF0000u : xf[s][0] + 0u, q0); q1 = sq2(xf[s][1] + 0u, q1); }
        if (!(EXP1 & 2) || s == 35) ab = MF32(asb(xf[s]), asb(wb), ab);
        if (!(EXP1 & 1) || s == 35) { q0 = sq2(xf[s][2] + 0u, q0); q1 = sq2(xf[s][3] + 0u, q1); }
        if (LAST && (!(EXP1 & 2) || s == 35)) ac = MF32(asb(wc), asb(xf[s]), ac);
        wa = na; wb = nb; wc = nc;
        SB();
    }
    ST(2);
    q0 += q1;
    { auto t = __builtin_amdgcn_permlane32_swap(__float_as_uint(q0), __float_as_uint(q0), false, false); q0 = __uint_as_float(t[0]) + __uint_as_float(t[1]); }
    const float rho = __builtin_amdgcn_rsqf(q0 * (1.f / 576.f) + 1e-5f);
    if (hh == 0) *(LAS float*)(lds + scw) = rho;
    if (r >= 18 && r < 22) {
#pragma unroll
        for (int i = 0; i < 4; ++i) *(LAS f32x4*)(lds + scl + 32 * i) = f32x4{ab[4 * i], ab[4 * i + 1], ab[4 * i + 2], ab[4 * i + 3]};
    }
    asm volatile("" ::: "memory");
    f32x4 rr[4];
#pragma unroll
    for (int i = 0; i < 4; ++i) rr[i] = *(const LAS f32x4*)(lds + scr + 32 * i);
    const float l0 = *(const LAS float*)(lds + scw + 128), l1 = *(const LAS float*)(lds + scw + 256), l2 = *(const LAS float*)(lds + scw + 384), l3 = *(const LAS float*)(lds + scw + 512);
    SB();
    const float rq = rho * QSCALE;
    const u32x4 kf = {pk(aa[8] * rho, aa[9] * rho), pk(aa[10] * rho, aa[11] * rho), pk(aa[12] * rho, aa[13] * rho), pk(aa[14] * rho, aa[15] * rho)};
    __builtin_amdgcn_raw_buffer_store_b128(kf, rk, ko, own, 16);
    *(LAS u32x4*)(lds + ko) = kf;
    u32x4 qf = {pk(aa[0] * rq, aa[1] * rq), pk(aa[2] * rq, aa[3] * rq), pk(aa[4] * rq, aa[5] * rq), pk(aa[6] * rq, aa[7] * rq)};
    float dg = aa[0] * aa[8], dh = aa[1] * aa[9];
#pragma unroll
    for (int i = 2; i < 8; i += 2) { dg = __builtin_fmaf(aa[i], aa[8 + i], dg); dh = __builtin_fmaf(aa[i + 1], aa[9 + i], dh); }
    dg += dh;
    { auto t = __builtin_amdgcn_permlane32_swap(__float_as_uint(dg), __float_as_uint(dg), false, false); dg = __uint_as_float(t[0]) + __uint_as_float(t[1]); }
    float nr = -rho * rq;
    asm volatile("" : "+v"(qf), "+v"(dg), "+v"(nr));
    SB();
    if (isv) {
#pragma unroll
        for (int s = 0; s < 2; ++s) {
            const u32x4 vf = {pk(ab[8 * s] * rr[2 * s][0], ab[8 * s + 1] * rr[2 * s][1]), pk(ab[8 * s + 2] * rr[2 * s][2], ab[8 * s + 3] * rr[2 * s][3]),
                              pk(ab[8 * s + 4] * rr[2 * s + 1][0], ab[8 * s + 5] * rr[2 * s + 1][1]), pk(ab[8 * s + 6] * rr[2 * s + 1][2], ab[8 * s + 7] * rr[2 * s + 1][3])};
            __builtin_amdgcn_raw_buffer_store_b128(vf, rk, vo + s * 576, own, 16);
            *(LAS u32x4*)(lds + vo + s * 576) = vf;
        }
    }
    const unsigned q67 = pk(l0 * rq, l1 * rq), k67 = pk(l2 * rho, l3 * rho);
    if (hh) {
        __builtin_amdgcn_raw_buffer_store_b32(k67, rk, k2o, own, 16);
        *(LAS unsigned*)(lds + k2o) = k67;
    }
    const unsigned nm = pk(__builtin_fmaf(l0, l2, __builtin_fmaf(l1, l3, dg)) * nr, 0.f);
    const u32x2 qa = {hh ? 0u : q67, hh ? 0u : nm}, qb = {hh ? q67 : 0u, hh ? nm : 0u};
    if (LAST) {
        __builtin_amdgcn_raw_buffer_store_b128(u32x4{pk(ac[0], ac[1]), pk(ac[2], ac[3]), pk(ac[4], ac[5]), pk(ac[6], ac[7])}, r3, oo, 0, 16);
        if (hh == 0) __builtin_amdgcn_raw_buffer_store_b32(pk(ac[8], ac[9]), r3, oo2, 0, 16);
    }
    f32x16 acc;
    {
        int tmp, n = 5, dm = w * 1024, m0s;
        asm volatile(
            "s_mov_b32 %[m0s], m0\n" A_BAR STP(0)
            "s_cmp_lt_u32 %[w], 4\n s_cbranch_scc1 20f\n" A_SIGNAL("21") "s_branch 22f\n 20:\n s_setprio 1\n 22:\n"
            A_PRIME0 A_S0F A_S1() A_S2() A_S3() A_S4()
            "s_cmp_lt_u32 %[w], 4\n s_cbranch_scc0 23f\n" A_SIGNAL("24") "23:\n"
            A_S5() A_S6() A_STEP("32", , , A_R128("72", "v87", "8640") A_NEXT) STP(1)
            "s_cmp_lt_u32 %[w], 4\n s_cbranch_scc0 25f\n s_setprio 0\n"
            A_POLL("3", "s_bitcmp1_b32 vcc_lo, 0\n") STP(2)
            "buffer_load_dwordx4 v[92:95], v89, %[rk], %[so] offen sc1\n s_add_u32 %[tmp], %[so], 0x1000\n"
            "buffer_load_dwordx4 v[96:99], v89, %[rk], %[tmp] offen sc1\n s_add_u32 %[tmp], %[so], 0x2000\n"
            "buffer_load_dwordx4 v[100:103], v89, %[rk], %[tmp] offen sc1\n s_add_u32 %[tmp], %[so], 0x3000\n"
            "buffer_load_dwordx4 v[104:107], v89, %[rk], %[tmp] offen sc1\n s_lshl_b32 %[tmp], %[w], 9\n s_sub_u32 %[tmp], 0x4000, %[tmp]\n"
            "v_lshrrev_b32 v110, 1, v89\n v_sub_u32 v111, v91, v110\n v_add_u32 v111, %[tmp], v111\n s_add_u32 %[tmp], %[so], %[tmp]\n"
            "buffer_load_dwordx2 v[108:109], v110, %[rk], %[tmp] offen sc1\n"
            "s_cmp_eq_u32 vcc_lo, -1\n s_cbranch_scc1 40f\n"
            A_POLL("4", "s_cmp_eq_u32 vcc_lo, -1\n")
            "s_waitcnt vmcnt(4)\n" A_WR(0, "v91", "0") "s_waitcnt vmcnt(3)\n" A_WR(1, "v91", "4096") "s_waitcnt vmcnt(2)\n" A_WR(2, "v91", "8192")
            "s_waitcnt vmcnt(1)\n" A_WR(3, "v91", "12288") "s_waitcnt vmcnt(0)\n ds_write_b64 v111, v[108:109]\n"
            "25:\n"
            "s_add_u32 %[so], %[so], 0x4800\n s_cmp_ge_u32 %[so], 0x24000\n s_cselect_b32 %[tmp], 0x24000, 0\n s_sub_u32 %[so], %[so], %[tmp]\n"
            "v_add_u32 v90, 0x10000, v91\n"
            A_BAR STP(3)
            A_GL(0) A_GL(1) A_GL(2)
            A_PR(3) A_PRIME A_S0(A_GL(3) A_GL(4) A_GL(5)) A_S1(A_GL(6) A_GL(7) A_GL(8)) A_PR(2) A_S2(A_GL(9) A_GL(10) A_GL(11)) A_PR(1) A_S3(A_GL(12) A_GL(13))
            A_PR(0) A_S4("s_waitcnt vmcnt(11)\n" A_WR(0, "v91", "18432") A_WR(1, "v91", "18432+8192") A_WR(2, "v91", "18432+2*8192"))
            A_BAR STP(4) A_PR(3) A_S567
            A_S0() A_PR(2) A_S1("s_waitcnt vmcnt(8)\n" A_WR(3, "v91", "18432+3*8192") A_WR(4, "v91", "18432+4*8192") A_WR(5, "v91", "18432+5*8192"))
            A_S2("s_waitcnt vmcnt(5)\n" A_WR(6, "v90", "18432+6*8192-65536") A_WR(7, "v90", "18432+7*8192-65536") A_WR(8, "v90", "18432+8*8192-65536"))
            A_PR(1) A_S3("s_waitcnt vmcnt(2)\n" A_WR(9, "v90", "18432+9*8192-65536") A_WR(10, "v90", "18432+10*8192-65536") A_WR(11, "v90", "18432+11*8192-65536"))
            A_PR(0) A_S4("s_waitcnt vmcnt(0)\n" A_WR(12, "v90", "18432+12*8192-65536") "s_cmp_lt_u32 %[w], 4\n s_cbranch_scc0 26f\n" A_WR(13, "v90", "18432+13*8192-65536") "26:\n")
            A_BAR STP(5) A_PR(3) A_S567
            "27:\n" A_PRIF(4, 2, "51") A_PRIF(3, 1, "52") A_PRIF(1, 2, "54") "s_cmp_eq_u32 %[n], 2\n s_cbranch_scc0 55f\n s_barrier\n s_setprio 3\n 55:\n"
            A_S0(A_DMA("60", A_D1)) A_S1(A_DMA("61", )) A_S2(A_DMA("62", )) A_S3(A_DMA("63", )) A_PRIF(1, 1, "57") A_S4(A_DMA("64", )) A_S567P
            "s_sub_u32 %[n], %[n], 1\n s_cmp_lg_u32 %[n], 0\n s_cbranch_scc1 27b\n"
            "s_waitcnt lgkmcnt(0)\n" A_PV("72", "48") "s_mov_b32 m0, %[m0s]\n s_nop 15\n"
            : "={v[0:15]}"(acc), "+{v85}"(kp), "+{v86}"(k2p), "+{v87}"(vp), "+{v90}"(foff), [so] "+s"(so), [tmp] "=&s"(tmp), [n] "+s"(n), [dm] "+s"(dm), [m0s] "=&s"(m0s)
            : "{v[76:79]}"(qf), "{v[80:81]}"(qa), "{v[82:83]}"(qb), "{v88}"(vstep), "{v91}"(pd), [rk] "s"(rk), [rw] "s"(rw), [fb] "s"(fb), [w] "s"(w),
              [lb] "s"(lb), [cnt] "s"(lb + L_CNT), [mine] "s"(mine)
            : "memory", "scc", "vcc", "v16", "v17", "v18", "v19", "v20", "v21", "v22", "v23", "v24", "v25", "v26", "v27", "v28", "v29", "v30", "v31", "v32", "v33", "v34", "v35",
              "v36", "v37", "v38", "v39", "v40", "v41", "v42", "v43", "v44", "v45", "v46", "v47", "v48", "v49", "v50", "v51", "v52", "v53", "v54", "v55", "v56", "v57", "v58",
              "v59", "v60", "v61", "v62", "v63", "v64", "v65", "v66", "v67", "v68", "v69", "v70", "v71", "v72", "v73", "v74", "v75", "v84", "v89", "v92", "v93", "v94",
              "v95", "v96", "v97", "v98", "v99", "v100", "v101", "v102", "v103", "v104", "v105", "v106", "v107", "v108", "v109", "v110", "v111", "v112", "v113", "v114", "v115",
              "v116", "v117", "v118", "v119", "v120", "v121", "v122", "v123", "v124", "v125", "v126", "v127", "v128", "v129", "v130", "v131", "v132", "v133", "v134", "v135",
              "v136", "v137", "v138", "v139", "v140", "v141", "v142", "v143", "v144", "v145", "v146", "v147");
    }
    ST(9);
    float l = hh ? acc[8] : acc[10];
    const f32x2 sm = (f32x2{acc[0], acc[1]} + f32x2{acc[2], acc[3]}) + (f32x2{acc[4], acc[5]} + f32x2{acc[6], acc[7]}) + f32x2{acc[8], acc[9]};
    bool ok = l > 1e-12f && l < 1e30f && fabsf(sm[0] + sm[1]) < 3e38f;
    if (__builtin_expect(!__all(ok), 0)) {
        acc = f32x16{};
        float m = -3.0e38f;
        const unsigned cv = (r == 18 || r == 20) ? 0x3F803F80u : 0u;
        for (int t = 0; t < 64; ++t) {
            const int co = (t >> 3) * (int)KVCH, tt = t & 7;
            const unsigned k2 = __builtin_amdgcn_raw_buffer_load_b32(rk, 8192 + lane * 4, co + (tt >> 1) * 256, 16);
            const u32x4 k1 = __builtin_amdgcn_raw_buffer_load_b128(rk, lane * 16, co + tt * 1024, 16);
            u32x4 v0 = {cv, cv, cv, cv}, v1 = v0;
            if (isv) {
                v0 = __builtin_amdgcn_raw_buffer_load_b128(rk, 9216 + hh * 288 + r * 16, co + tt * 1152, 16);
                v1 = __builtin_amdgcn_raw_buffer_load_b128(rk, 9792 + hh * 288 + r * 16, co + tt * 1152, 16);
            }
            f32x16 sA = MF32(asb(k1), asb(qf), f32x16{});
            sA = MF32(asb(u32x4{k2, 0u, 0u, 0u}), asb(u32x4{(tt & 1) ? qb[0] : qa[0], 0u, 0u, 0u}), sA);
            const float mn = fmaxf(m, max16(sA)), al = EX(m - mn);
            m = mn;
#pragma unroll
            for (int i = 0; i < 16; ++i) acc[i] *= al;
            f32x8 p0, p1;
#pragma unroll
            for (int i = 0; i < 8; ++i) { p0[i] = EX(sA[i] - mn); p1[i] = EX(sA[8 + i] - mn); }
            acc = __builtin_amdgcn_smfmac_f32_32x32x32_bf16(asb(v0), __builtin_bit_cast(bf16x16, p0), acc, (int)0xDDDDDDDDu, 0, 0);
            acc = __builtin_amdgcn_smfmac_f32_32x32x32_bf16(asb(v1), __builtin_bit_cast(bf16x16, p1), acc, (int)0xDDDDDDDDu, 0, 0);
        }
        l = hh ? acc[8] : acc[10];
    }
    const float il = __builtin_amdgcn_rcpf(l);
    __builtin_amdgcn_raw_buffer_store_b128(u32x4{pk(acc[0] * il, acc[1] * il), pk(acc[2] * il, acc[3] * il), pk(acc[4] * il, acc[5] * il), pk(acc[6] * il, acc[7] * il)}, ro, oo, 0, 16);
    if (hh == 0) __builtin_amdgcn_raw_buffer_store_b32(pk(acc[8] * il, acc[9] * il), ro, oo2, 0, 16);
    const int a = h & 7, p = lane & 15, g = lane >> 4, kh = w >> 2, orow = b * 256 + (h >> 3) * 64 + (w & 3) * 16 + p;
    gu32* of = (gu32*)(ws + O_FLG + (LAST ? 98304 : 32768) + b * 4096);
    u32x4 e0 = {}, e1 = {}; u32x2 e2 = {};
    auto residual = [&]() {
        if (kh == 0) {
            const rsrc_t rs = LAST ? r3 : rx;
            e0 = __builtin_amdgcn_raw_buffer_load_b128(rs, (g * S + orow) * 16, 9 * a * (S * 16), 16);
            e1 = __builtin_amdgcn_raw_buffer_load_b128(rs, (g * S + orow) * 16, (9 * a + 4) * (S * 16), 16);
            e2 = __builtin_amdgcn_raw_buffer_load_b64(rs, orow * 16 + (g & 1) * 8, (9 * a + 8) * (S * 16), 16);
        }
    };
    if (!LAST) residual();
    ST(10);
    asm volatile("s_waitcnt vmcnt(0)\n\ts_barrier" ::: "memory");
    ST(11);
    if (tid == 0) __hip_atomic_store(of + h * 32, 1u, __ATOMIC_RELAXED, __HIP_MEMORY_SCOPE_AGENT);
    u32x4 wf[9][5];
    {
        const char* pw = lds + kh * 41472 + lane * 16;
        const char* p4 = lds + kh * 41472 + 4096 + g * 128 + (lane & 7) * 16;
#pragma unroll
        for (int s = 0; s < 9; ++s) {
#pragma unroll
            for (int T = 0; T < 4; ++T) wf[s][T] = lds128(pw + s * 4608 + T * 1024);
            wf[s][4] = lds128(p4 + s * 4608);
        }
    }
    if (w == 1) {
#pragma nounroll
        for (int spin = 0; spin < 0x200000; ++spin) {
            if (__all(__hip_atomic_load(of + r * 32, __ATOMIC_RELAXED, __HIP_MEMORY_SCOPE_AGENT) == 1u)) break;
            __builtin_amdgcn_s_sleep(1);
        }
    }
    asm volatile("s_barrier" ::: "memory");
    ST(12);
    u32x4 rf[9];
    if (LAST) residual();
#pragma unroll
    for (int s = 0; s < 9; ++s) rf[s] = __builtin_amdgcn_raw_buffer_load_b128(ro, (g * S + orow) * 16, (9 * kh + s) * (4 * S * 16), 16);
    f32x4 oc[5] = {};
#pragma unroll
    for (int s = 0; s < 9; ++s)
#pragma unroll
        for (int T = 0; T < 5; ++T) oc[T] = MF16(asb(wf[s][T]), asb(rf[s]), oc[T]);
    char* xc = lds + 98304 + (w & 3) * 5120 + lane * 16;
    if (kh) {
#pragma unroll
        for (int T = 0; T < 5; ++T) *(LAS f32x4*)(xc + T * 1024) = oc[T];
    }
    ST(13);
    asm volatile("s_waitcnt lgkmcnt(0)\n\ts_barrier" ::: "memory");
    if (kh == 0) {
        const unsigned e[10] = {e0[0], e0[1], e0[2], e0[3], e1[0], e1[1], e1[2], e1[3], e2[0], e2[1]};
#pragma unroll
        for (int T = 0; T < 5; ++T) oc[T] += *(const LAS f32x4*)(xc + T * 1024) + f32x4{blo(e[2 * T]), bhi(e[2 * T]), blo(e[2 * T + 1]), bhi(e[2 * T + 1])};
        if (!LAST) store_xh(oc, out, ws + O_SS2, a, orow, g);
        else {
            char* d = out + ((size_t)orow * D + 72 * a) * 2;
#pragma unroll
            for (int u = 0; u < 2; ++u)
                *(u32x4*)(d + (32 * u + 8 * g) * 2) = u32x4{pk(oc[2 * u][0], oc[2 * u][1]), pk(oc[2 * u][2], oc[2 * u][3]), pk(oc[2 * u + 1][0], oc[2 * u + 1][1]), pk(oc[2 * u + 1][2], oc[2 * u + 1][3])};
            if (g < 2) *(u32x2*)(d + 128 + g * 8) = u32x2{pk(oc[4][0], oc[4][1]), pk(oc[4][2], oc[4][3])};
        }
    }
    ST(14);
}

__global__ __launch_bounds__(512, 2) void k_qa0(char* ws) { qa_body<false>(ws, ws + O_XH2); }
__global__ __launch_bounds__(512, 2) void k_qa1(char* ws, char* out) {
    qa_body<true>(ws, out);
    asm volatile("text_end_mark:");
}

extern "C" void kernel_launch(void* const* d_in, const int* in_sizes, int n_in, void* d_out, int out_size, void* d_ws, size_t ws_size, hipStream_t stream) {
    const u16* x = (const u16*)d_in[0]; const u16* wr = (const u16*)d_in[1]; const u16* wi = (const u16*)d_in[2];
    const u16* wqkv = (const u16*)d_in[4]; const u16* wo = (const u16*)d_in[5];
    char* ws = (char*)d_ws;
    k_in<<<B_END, 256, 0, stream>>>(x, wr, wi, wqkv, wo, ws);
    k_fft<<<256, 512, 0, stream>>>(ws + O_XH0, ws + O_XH1, ws + O_SS0, ws, wqkv, wo);
    k_qa0<<<256, 512, 0, stream>>>(ws);
    k_fft<<<256, 512, 0, stream>>>(ws + O_XH2, ws + O_XH3, ws + O_SS2, ws, nullptr, wo);
    k_qa1<<<256, 512, 0, stream>>>(ws, (char*)d_out);
}
```

```cpp
#include <hip/hip_runtime.h>
#include <hip/hip_bf16.h>
#include <stdint.h>
#include <utility>

typedef __attribute__((ext_vector_type(8))) __bf16 bf16x8;
typedef __attribute__((ext_vector_type(2))) __bf16 bf16x2;
typedef __attribute__((ext_vector_type(8))) _Float16 f16x8;
typedef __attribute__((ext_vector_type(2))) _Float16 f16x2;
typedef __attribute__((ext_vector_type(2))) float f32x2;
typedef __attribute__((ext_vector_type(4))) float f32x4;
typedef __attribute__((ext_vector_type(16))) float f32x16;
typedef __attribute__((ext_vector_type(8))) float f32x8;
typedef __attribute__((ext_vector_type(16))) __bf16 bf16x16;
typedef __attribute__((ext_vector_type(4))) unsigned u32x4;
typedef __attribute__((ext_vector_type(2))) unsigned u32x2;
typedef __attribute__((ext_vector_type(3))) unsigned u32x3;
typedef __attribute__((ext_vector_type(8))) unsigned u32x8;
typedef __attribute__((ext_vector_type(12))) unsigned u32x12;
typedef __attribute__((ext_vector_type(32))) unsigned u32x32;
typedef unsigned short u16;
typedef __attribute__((address_space(1))) unsigned gu32;
typedef __amdgpu_buffer_rsrc_t rsrc_t;
#define LAS __attribute__((address_space(3)))

constexpr int S = 2048, D = 576;

namespace tbl {
constexpr double PI = 3.14159265358979323846264338327950288;
constexpr double ccos(int m) {
    if (m > 288) m = 576 - m;
    double sg = 1.0;
    if (m > 144) { m = 288 - m; sg = -1.0; }
    if (m == 144) return 0.0;
    const double x = 2.0 * PI * m / 576.0, x2 = x * x;
    double term = 1.0, sum = 1.0;
    for (int i = 1; i < 16; ++i) { term *= -x2 / ((2 * i - 1) * (2 * i)); sum += term; }
    return sg * sum;
}
constexpr u16 tobf(double v) {
    const unsigned u = __builtin_bit_cast(unsigned, (float)v);
    return (u16)((u + 0x7FFFu + ((u >> 16) & 1u)) >> 16);
}
constexpr u16 tohf(double v) {
    const unsigned u = __builtin_bit_cast(unsigned, (float)v), a = u & 0x7FFFFFFFu;
    return a ? (u16)(((u >> 16) & 0x8000u) | ((a + 0xFFFu + ((a >> 13) & 1u) - (112u << 23)) >> 13)) : (u16)0;
}
struct Base { u16 c[576], h[576]; };
constexpr double SC = 0.05892556509887896;
constexpr Base mkbase() { Base b{}; for (int m = 0; m < 576; ++m) { b.c[m] = tobf(SC * ccos(m)); b.h[m] = tohf(SC * ccos(m)); } return b; }
constexpr Base base = mkbase();
constexpr u16 DCV = tobf(1.0 / 24.0), NDCV = tobf(-1.0 / 24.0);
constexpr int freq(int ch) {
    const int kb = ch >> 2, cc = ch & 3;
    const int w = kb < 64 ? kb >> 4 : (kb - 64) >> 1, g = kb < 64 ? kb & 3 : 2 * (kb & 1) + (cc >> 1), tau = kb < 64 ? 2 * ((kb >> 2) & 3) + (cc >> 1) : 8;
    const int sg = 3 * w + tau / 3, d = 8 * (tau % 3) + 2 * g + (cc & 1);
    if (sg) return d < 12 ? sg + 24 * d : 576 - sg - 24 * d;
    return d < 12 ? 24 * d : 12 + 24 * (d - 12);
}
constexpr u16 Fval(int n, int c) {
    if (c < 2) return (c == 0 || !(n & 1)) ? DCV : NDCV;
    return base.c[(freq(c >> 1) * n + ((c & 1) ? 144 : 0)) % 576];
}
struct Chunk { u16 v[4608]; };
template <int I> constexpr Chunk mkF2() {
    Chunk c{}; int o = 0;
    for (int s = 9 * (I & 3); s < 9 * (I & 3) + 9; ++s) for (int hh = 0; hh < 2; ++hh) for (int m = 0; m < 32; ++m)
        for (int e = 0; e < 8; ++e) c.v[o++] = Fval(72 * (I >> 3) + 8 * (I & 4) + (m & 19) + 8 * ((m >> 2) & 1) + 4 * ((m >> 3) & 1), 16 * s + 8 * hh + e);
    return c;
}
constexpr Chunk mkT2() {
    Chunk c{}; int o = 0;
    for (int sp = 0; sp < 2; ++sp) for (int T = 0; T < 3; ++T) for (int t = 0; t < 2; ++t) for (int l = 0; l < 64; ++l) for (int e = 0; e < 6; ++e) {
        const int m = l & 15, d = 8 * T + (m >> 1), po = m & 1, b = 6 * (l >> 4) + 3 * t + (e >> 1), pi = e & 1;
        u16 v = 0;
        if (!sp) v = base.h[(24 * b * d + (po ? (pi ? 0 : 144) + (d >= 12 ? 288 : 0) : (pi ? 432 : 0))) % 576];
        else if (d == 0) v = pi ? (u16)0 : tohf((po && (b & 1)) ? -1.0 / 24.0 : 1.0 / 24.0);
        else if (d < 12) v = pi ? (u16)0 : base.h[(24 * b * d + 144 * po) % 576];
        else v = pi ? base.h[((12 + 24 * (d - 12)) * b + 144 * po) % 576] : (u16)0;
        c.v[o++] = v; }
    return c;
}
template <int I> constexpr Chunk f2c = mkF2<I>();
struct Table { Chunk c[64]; };
template <int... I> constexpr Table mkF2T(std::integer_sequence<int, I...>) { return Table{{f2c<I>...}}; }
}
__device__ __attribute__((aligned(256))) const tbl::Table F2H = tbl::mkF2T(std::make_integer_sequence<int, 64>{});
__device__ __attribute__((aligned(256))) const tbl::Chunk TFF = tbl::mkT2();

constexpr size_t CELLS = (size_t)72 * S * 16;
constexpr size_t GIMG = 83968;
constexpr size_t HIMG = 64512;
constexpr size_t KVCH = 18432;
constexpr size_t al(size_t x) { return (x + 4095) & ~(size_t)4095; }
constexpr size_t PLANES = (size_t)146 * 16384;
constexpr size_t O_XH0 = 0, O_XH1 = O_XH0 + PLANES;
constexpr size_t O_OA = O_XH1 + CELLS;
constexpr size_t O_XH2 = O_OA + CELLS, O_XH3 = O_XH2 + PLANES, O_OB = O_XH3 + CELLS, O_X3 = O_OB + CELLS;
constexpr size_t O_WT = O_X3 + CELLS;
constexpr size_t O_WO2 = O_WT + (size_t)288 * 8192;
constexpr size_t O_WOH = O_WO2 + al(8 * GIMG + 4096);
constexpr size_t O_WQH = O_WOH + al(8 * GIMG + 4096);
constexpr size_t O_KVA = O_WQH + al(32 * HIMG + 4096), O_KVB = O_KVA + 256 * KVCH;
constexpr size_t O_SS = O_KVB + 256 * KVCH;
constexpr size_t O_FLG = O_SS + 65536;
constexpr int F_RDY = 131072, F_EP = 262144;
constexpr size_t O_END = O_FLG + 270336;
constexpr size_t O_KF = O_END;

__device__ __forceinline__ unsigned pk(float a, float b) { f32x2 t = {a, b}; return __builtin_bit_cast(unsigned, __builtin_convertvector(t, bf16x2)); }
__device__ __forceinline__ unsigned pkh(float a, float b) { f32x2 t = {a, b}; return __builtin_bit_cast(unsigned, __builtin_convertvector(t, f16x2)); }
__device__ __forceinline__ float hlo(unsigned u) { return (float)__builtin_bit_cast(f16x2, u)[0]; }
__device__ __forceinline__ float hhi(unsigned u) { return (float)__builtin_bit_cast(f16x2, u)[1]; }
__device__ __forceinline__ float blo(unsigned u) { return __uint_as_float(u << 16); }
__device__ __forceinline__ float bhi(unsigned u) { return __uint_as_float(u & 0xFFFF0000u); }
__device__ __forceinline__ float sq2(unsigned v, float q) { const bf16x2 t = __builtin_bit_cast(bf16x2, v); return __builtin_amdgcn_fdot2_f32_bf16(t, t, q, false); }
__device__ __forceinline__ float sum2(unsigned v, float q) { return __builtin_amdgcn_fdot2_f32_bf16(__builtin_bit_cast(bf16x2, v), __builtin_bit_cast(bf16x2, 0x3F803F80u), q, false); }
__device__ __forceinline__ rsrc_t mkrsrc(const void* p, int bytes) { return __builtin_amdgcn_make_buffer_rsrc((void*)p, (short)0, bytes, 0x00020000); }
constexpr unsigned MK = 0xFFFFFFFEu;
__device__ __forceinline__ int mu(int c) { return ((c & 2) << 2) | ((c & 12) >> 1) | (c & 1); }
__device__ __forceinline__ int unmu(int p) { return ((p & 8) >> 2) | ((p & 6) << 1) | (p & 1); }
__device__ __forceinline__ int nrow(int rho) { return 32 * ((rho & 48) | unmu(rho & 15)) + (rho >> 6); }
__device__ __forceinline__ void st16(char* ws, unsigned so, unsigned off, u32x4 v) {
    asm volatile("buffer_store_dwordx4 %0, %1, %2, %3 offen sc1\n s_nop 1" :: "v"(v), "v"(off), "s"(mkrsrc(ws, (int)O_END)), "s"(so) : "memory");
}
__device__ __forceinline__ void st8(char* ws, unsigned so, unsigned off, u32x2 v) { __builtin_amdgcn_raw_buffer_store_b64(v, mkrsrc(ws, (int)O_END), off, so, 16); }
__device__ __forceinline__ void st4(char* ws, unsigned so, unsigned off, unsigned v) { __builtin_amdgcn_raw_buffer_store_b32(v, mkrsrc(ws, (int)O_END), off, so, 16); }
__device__ __forceinline__ f16x8 ash(u32x4 v) { return __builtin_bit_cast(f16x8, v); }
__device__ __forceinline__ bf16x8 asb(u32x4 v) { return __builtin_bit_cast(bf16x8, v); }
#define MF16(a, b, c) __builtin_amdgcn_mfma_f32_16x16x32_bf16(a, b, c, 0, 0, 0)
#define MH16(a, b, c) __builtin_amdgcn_mfma_f32_16x16x32_f16(a, b, c, 0, 0, 0)
#define MF32(a, b, c) __builtin_amdgcn_mfma_f32_32x32x16_bf16(a, b, c, 0, 0, 0)
#define SB() __builtin_amdgcn_sched_barrier(0)
__device__ __forceinline__ u32x4 lds128(const char* p) { return *(const LAS u32x4*)(p); }
__device__ __forceinline__ unsigned gimg_off(int kb, int c) {
    const int a = c / 72, x = c - 72 * a, kh = kb < 64 ? kb >> 5 : (kb >> 2) & 1, s = kb < 64 ? (kb >> 2) & 7 : 8, g = kb & 3;
    unsigned o;
    if (x < 64) { const int T = 2 * (x >> 5) + ((x >> 2) & 1), p = 4 * ((x >> 3) & 3) + (x & 3); o = (4 * s + T) * 1024 + p * 16; }
    else o = (36 + (s >> 1)) * 1024 + ((s & 1) * 8 + x - 64) * 16;
    return a * (unsigned)GIMG + kh * 41984 + g * 256 + o;
}
__device__ __forceinline__ int ofeat(int kb, int e) {
    return kb < 64 ? 18 * (kb >> 1) + (e & 3) + 8 * (e >> 2) + 4 * (kb & 1) + (kb & 2) : 18 * (4 * (kb - 64) + (e >> 1)) + 16 - 8 * (e & 2) + (e & 1);
}

__device__ __forceinline__ float sum16(float q) {
    q += __uint_as_float(__builtin_amdgcn_mov_dpp(__float_as_uint(q), 0x128, 15, 15, true));
    q += __uint_as_float(__builtin_amdgcn_mov_dpp(__float_as_uint(q), 0x124, 15, 15, true));
    q += __uint_as_float(__builtin_amdgcn_mov_dpp(__float_as_uint(q), 0x122, 15, 15, true));
    return q + __uint_as_float(__builtin_amdgcn_mov_dpp(__float_as_uint(q), 0x121, 15, 15, true));
}
__device__ __forceinline__ float sum4g(float q) {
    auto t = __builtin_amdgcn_permlane16_swap(__float_as_uint(q), __float_as_uint(q), false, false);
    q = __uint_as_float(t[0]) + __uint_as_float(t[1]);
    t = __builtin_amdgcn_permlane32_swap(__float_as_uint(q), __float_as_uint(q), false, false);
    return __uint_as_float(t[0]) + __uint_as_float(t[1]);
}

constexpr int B16 = 60, WOD = 40;
constexpr int ATT_CONST = 8 * (int)KVCH;
constexpr int L_W = 2 * (int)KVCH, L_F2 = L_W + 65536, L_SC = (int)KVCH, L_CNT = ATT_CONST + 10240, L_FFT = 20480;
constexpr int L_END = L_CNT + 32;
__device__ __forceinline__ u32x2 mktag(unsigned long long e) { return __builtin_bit_cast(u32x2, e * 6364136223846793005ull + 1442695040888963407ull); }
__device__ __forceinline__ unsigned arrive(unsigned cnt) {
    unsigned c;
    asm volatile("s_mov_b64 exec, 1\n ds_add_rtn_u32 %0, %1, %2\n s_mov_b64 exec, -1\n s_waitcnt lgkmcnt(0)" : "=&v"(c) : "v"(cnt), "v"(1u) : "memory");
    return __builtin_amdgcn_readfirstlane(c);
}
__device__ __forceinline__ void await(unsigned cnt, unsigned n) {
#pragma nounroll
    for (int spin = 0; spin < 0x100000; ++spin) {
        unsigned c;
        asm volatile("ds_read_b32 %0, %1\n s_waitcnt lgkmcnt(0)" : "=v"(c) : "v"(cnt) : "memory");
        if (__builtin_amdgcn_readfirstlane(c) == n) break;
    }
}
template <int N = 1> __device__ __forceinline__ void post(char* ws, int slot, int lane, u32x2 tag, unsigned now) { st16(ws, O_FLG + F_RDY + slot * 128, lane < N ? lane * 16 : 0x40000000, u32x4{tag[0], tag[1], now, 0u}); }
__device__ __forceinline__ unsigned fft576a(int MODE, char* __restrict__ ws, char* lds, int vb, int w, int lane, u32x3 (&raw)[6], u32x3 (&t2)[6], u32x3 (&t0)[6], u32x2& dq, unsigned cnt,
                                            unsigned long long& ep) {
    const int v = lane & 15, g = lane >> 4;
    u32x4 df[6];
    const rsrc_t rt = mkrsrc(&TFF, sizeof(TFF));
#pragma unroll
    for (int i = 0; i < 6; ++i) asm volatile("buffer_load_dwordx3 %0, %1, %2, 0 offen offset:%3" : "=v"(t2[i]) : "v"(lane * 12), "s"(rt), "n"(i * 768));
    if (w == 0) {
#pragma unroll
        for (int i = 0; i < 6; ++i) asm volatile("buffer_load_dwordx3 %0, %1, %2, %3 offen offset:%4" : "=v"(t0[i]) : "v"(lane * 12), "s"(rt), "s"(4608), "n"(i * 768));
    }
    if (MODE != 2) asm volatile("s_barrier" ::: "memory");
    u32x4 t1[12], t1l[12];
    {
        const float f = (float)v * (1.f / 576.f), b0 = f * (float)(24 * g + 6 * w), b1 = (v ? 0.f : (g & 1) ? -0.25f : 0.25f) - b0;
        const float k2 = __builtin_rintf(2.f * __builtin_amdgcn_cosf((float)v * (1.f / 6.f)));
        const unsigned sh = (unsigned)lane << 31, sg = sh | (sh >> 16);
#pragma unroll
        for (int bl = 0; bl < 6; ++bl) {
            const float c0 = __builtin_amdgcn_cosf(__builtin_fmaf(f, (float)bl, b0)), c1 = __builtin_amdgcn_cosf(__builtin_fmaf(f, (float)(96 + bl), b0));
            const float s0 = __builtin_amdgcn_sinf(__builtin_fmaf(f, (float)-bl, b1)), s1 = __builtin_amdgcn_sinf(__builtin_fmaf(f, (float)-(96 + bl), b1));
            const float c2 = __builtin_fmaf(k2, c1, -c0), s2 = __builtin_fmaf(k2, s1, -s0);
            const unsigned pc = pk(c0, c1), ps = pk(s0, s1), qc = pk(c2, c2), qs = pk(s2, s2);
            t1[2 * bl] = u32x4{pc, pc ^ sg, qc ^ sh, 0u};
            t1[2 * bl + 1] = u32x4{ps, ps ^ sg, qs ^ sh, 0u};
            const unsigned lc = pk(c0 - blo(pc), c1 - bhi(pc)), ls = pk(s0 - blo(ps), s1 - bhi(ps));
            t1l[2 * bl] = u32x4{lc, lc ^ sg, pk(c2 - bhi(qc), c2 - bhi(qc)) ^ sh, 0u};
            t1l[2 * bl + 1] = u32x4{ls, ls ^ sg, pk(s2 - bhi(qs), s2 - bhi(qs)) ^ sh, 0u};
            asm volatile("" : "+v"(t1[2 * bl]), "+v"(t1[2 * bl + 1]), "+v"(t1l[2 * bl]), "+v"(t1l[2 * bl + 1]));
        }
    }
    if (MODE == 1) {
        const int sec = vb / 36, c = vb * 16 + v - 576 * sec, h = sec < 3 ? (c * 3641) >> 16 : c >> 1, dm = c - 18 * h - 2 * (h & 1);
        const bool qk = sec < 2 && (unsigned)dm < 16u;
        const int hs = qk ? 512 : 384;
        const unsigned d = h * (int)HIMG + ((unsigned)dm < 16u && sec < 3 ? (sec * sec * 16 + dm) * 16 : 1280 + (sec == 2 ? 0 : sec == 3 ? 96 : 32 + sec * 32) + (v & 1) * 16);
        dq = u32x2{d + (g >> 1) * 1792 + (g & 1) * hs, (g & 1) ? 0x40000000 : d + (g >> 1) * hs};
        asm volatile("" : "+v"(dq));
    }
    asm volatile("s_waitcnt vmcnt(6)" : "+v"(raw[0]), "+v"(raw[1]), "+v"(raw[2]), "+v"(raw[3]), "+v"(raw[4]), "+v"(raw[5]) :: "memory");
    if (MODE == 0) {
        if (arrive(cnt - 16) != 3u) await(cnt - 16, 4u);
        const char* a = lds + 24576 + v * 1168 + g * 48 + w * 12;
#pragma unroll
        for (int e = 0; e < 6; ++e)
#pragma unroll
            for (int i = 0; i < 3; ++i) raw[e][i] = *(const LAS unsigned*)(a + e * 192 + i * 4);
    }
    if (MODE != 1) {
#pragma unroll
        for (int bp = 0; bp < 3; ++bp)
#pragma unroll
            for (int k = 0; k < 3; ++k) {
                const int e0 = k < 2 ? 3 * k : 2, e1 = k < 2 ? 3 * k + 1 : 5;
                df[2 * bp][k] = __builtin_amdgcn_perm(raw[e1][bp], raw[e0][bp], 0x05040100u);
                df[2 * bp + 1][k] = __builtin_amdgcn_perm(raw[e1][bp], raw[e0][bp], 0x07060302u);
            }
    } else {
        u32x2 p[9];
#pragma unroll
        for (int i = 0; i < 9; ++i) asm volatile("ds_read_b64_tr_b16 %0, %1 offset:%2" : "=v"(p[i]) : "v"((unsigned)(uintptr_t)lds + 24576 + w * 5120 + lane * 8), "n"(i * 512));
        asm volatile("s_waitcnt lgkmcnt(0)" : "+v"(p[0]), "+v"(p[1]), "+v"(p[2]), "+v"(p[3]), "+v"(p[4]), "+v"(p[5]), "+v"(p[6]), "+v"(p[7]), "+v"(p[8]));
#pragma unroll
        for (int bl = 0; bl < 6; ++bl) df[bl] = u32x4{p[bl][0], p[bl][1], p[6 + (bl >> 1)][bl & 1], 0u};
        if (__builtin_expect(vb >= 108, 0)) {
            int a = 6 * (vb & 3) + 3 * (v >> 3) + 2;
            asm volatile("" : "+v"(a));
            const int e = a >> 2;
#pragma unroll
            for (int bl = 0; bl < 6; ++bl) {
                const unsigned one = (g == (a & 3) && 16 + (v & 7) - 6 * w == bl) ? (e == 1 || e > 3 ? 0x3F800000u : 0x3F80u) : 0u;
                df[bl] = u32x4{e < 2 ? one : 0u, e == 3 || e == 4 ? one : 0u, e == 2 || e == 5 ? one : 0u, 0u};
            }
        }
    }
    unsigned y[4][6];
#pragma unroll
    for (int bl = 0; bl < 6; ++bl) {
        df[bl][3] = 0u;
        const f32x4 re = MF16(asb(t1[2 * bl]), asb(df[bl]), MF16(asb(t1l[2 * bl]), asb(df[bl]), f32x4{})), im = MF16(asb(t1[2 * bl + 1]), asb(df[bl]), MF16(asb(t1l[2 * bl + 1]), asb(df[bl]), f32x4{}));
#pragma unroll
        for (int r = 0; r < 4; ++r) y[r][bl] = pkh(re[r], im[r]);
    }
    if (g < 3) {
#pragma unroll
        for (int r = 0; r < 4; ++r)
#pragma unroll
            for (int t = 0; t < 2; ++t) *(LAS u32x4*)(lds + (4 * g + r) * 2048 + t * 1024 + w * 256 + v * 16) = u32x4{y[r][3 * t], y[r][3 * t + 1], y[r][3 * t + 2], 0u};
    }
    asm volatile("s_waitcnt vmcnt(0)" : "+v"(t2[0]), "+v"(t2[1]), "+v"(t2[2]), "+v"(t2[3]), "+v"(t2[4]), "+v"(t2[5]),
                 "+v"(t0[0]), "+v"(t0[1]), "+v"(t0[2]), "+v"(t0[3]), "+v"(t0[4]), "+v"(t0[5]));
    const unsigned now = (unsigned)__builtin_amdgcn_s_memrealtime(), c = arrive(cnt);
    asm volatile("" : "+s"(ep));
    if (c == 7u) post(ws, vb + 16, lane, mktag(ep), now);
    return c;
}
__device__ __forceinline__ void fft576b(int MODE, char* __restrict__ ws, char* lds, int vb, int w, int lane, u32x3 (&t2)[6], u32x3 (&t0)[6], u32x2 dq, unsigned cnt, unsigned c) {
    const int v = lane & 15, g = lane >> 4, vec = vb * 16 + v;
    u32x4 m2[6], m0[6];
#pragma unroll
    for (int i = 0; i < 6; ++i) { m2[i] = u32x4{t2[i][0], t2[i][1], t2[i][2], 0u}; m0[i] = u32x4{t0[i][0], t0[i][1], t0[i][2], 0u}; asm volatile("" : "+v"(m2[i]), "+v"(m0[i])); }
    if (c != (MODE ? 3u : 7u)) await(cnt, MODE ? 4u : 8u);
    f32x4 acc[9];
    u32x4 y0[3], y1[3];
    const float rho = *(const LAS float*)(lds + 43264 + v * 4);
#pragma unroll
    for (int sl = 0; sl < 3; ++sl) { y0[sl] = lds128(lds + (3 * w + sl) * 2048 + lane * 16); y1[sl] = lds128(lds + (3 * w + sl) * 2048 + 1024 + lane * 16); }
    auto stage2 = [&](bool tr, int sl, const u32x4 (&m)[6]) {
#pragma unroll
        for (int T = 0; T < 3; ++T) {
            if (!tr) acc[3 * sl + T] = MH16(ash(m[2 * T + 1]), ash(y1[sl]), MH16(ash(m[2 * T]), ash(y0[sl]), f32x4{}));
            else acc[3 * sl + T] = MH16(ash(y1[sl]), ash(m[2 * T + 1]), MH16(ash(y0[sl]), ash(m[2 * T]), f32x4{}));
        }
    };
    if (MODE != 2) {
        if (w == 0) { stage2(0, 0, m0); asm volatile("" ::: "memory"); } else stage2(0, 0, m2);
        stage2(0, 1, m2); stage2(0, 2, m2);
    } else {
        asm volatile("" ::: "memory");
        if (w == 0) { stage2(1, 0, m0); asm volatile("" ::: "memory"); } else stage2(1, 0, m2);
        stage2(1, 1, m2); stage2(1, 2, m2);
    }
    u32x2 o[9];
    if (MODE == 0) {
#pragma unroll
        for (int i = 0; i < 9; ++i) { acc[i] *= rho; o[i] = u32x2{pkh(acc[i][0], acc[i][1]), pkh(acc[i][2], acc[i][3])}; }
#pragma unroll
        for (int i = 0; i < 8; ++i) st8(ws, O_XH0 + (8 * (i >> 1) + (i & 1)) * 16384, vec * 8 + (32 * w + 2 * g) * 16384, u32x2{min(o[i][0], MK), min(o[i][1], MK)});
        st8(ws, O_XH0, vec * 8 + (128 + 4 * w + g) * 16384, u32x2{min(o[8][0], MK), min(o[8][1], MK)});
    } else {
#pragma unroll
        for (int i = 0; i < 9; ++i) o[i] = u32x2{pk(acc[i][0], acc[i][1]), pk(acc[i][2], acc[i][3])};
        auto cell = [&](u32x2 a, u32x2 b) {
            const auto x = __builtin_amdgcn_permlane16_swap(a[0], b[0], false, false), y = __builtin_amdgcn_permlane16_swap(a[1], b[1], false, false);
            return u32x4{x[0], y[0], x[1], y[1]};
        };
        if (MODE == 1) {
            if (w == 0 && g == 0 && vb < 108) o[0][0] &= 0xFFFF0000u;
#pragma unroll
            for (int u = 0; u < 4; ++u) st16(ws, O_WQH + (8 * w + 2 * u) * 1792, dq[0], u32x4{o[2 * u][0], o[2 * u][1], o[2 * u + 1][0], o[2 * u + 1][1]});
            st16(ws, O_WQH + (32 + w) * 1792, dq[1], cell(o[8], o[8]));
        } else {
#pragma unroll
            for (int u = 0; u < 4; ++u) st16(ws, O_WOH, gimg_off(2 * vb + (g >> 1), 8 * (16 * w + 4 * u + (v >> 2)) + 4 * (g & 1) + (v & 3)), cell(o[2 * u], o[2 * u + 1]));
            st16(ws, O_WOH, (g & 1) ? 0x40000000 : gimg_off(2 * vb + (g >> 1), 512 + 16 * w + v), cell(o[8], o[8]));
        }
    }
}
__device__ __forceinline__ void touch(const char* p, int bytes, int t, int nt) {
    unsigned d = 0;
    for (int o = t * 128; o < bytes; o += nt * 128) asm volatile("global_load_dword %0, %1, off" : "+v"(d) : "v"(p + o) : "memory");
    asm volatile("s_waitcnt vmcnt(0)" : "+v"(d));
}

__device__ __forceinline__ void done(char* ws, unsigned lb, int bx, int w, int lane, u32x2 tag) {
    const unsigned now = (unsigned)__builtin_amdgcn_s_memrealtime();
    asm volatile("s_waitcnt vmcnt(0)" ::: "memory");
    if (arrive(lb + L_CNT + 4 + (w & 4)) == 3u) post<8>(ws, (w & 4) * 64 + bx, lane, tag, now);
}
__device__ __forceinline__ unsigned filter_base(int vb) { asm("" : "+s"(vb)); return O_WT + (vb >> 6) * 4096 + (vb & 12) * 256 + (vb & 48) * 16 + (vb & 1) * 128; }
__device__ __forceinline__ void filter_where(unsigned (&d)[5], int vb, int i4, int lane) {
#pragma unroll
    for (int u = 0; u < 5; ++u) {
        const int j1 = 72 * (vb & 2) + 36 * i4 + 8 * u + 1 + (lane >> 3), jj = j1 == 288 ? 0 : j1, q = (jj * 2731) >> 16, m = jj - 24 * q;
        const int sg = m > 12 ? 24 - m : m == 12 ? 0 : m, dd = m > 12 ? 23 - q : m == 12 ? 12 + q : q;
        const int ww = (sg * 43) >> 7, tau = 3 * (sg - 3 * ww) + (dd >> 3), gg = (dd >> 1) & 3;
        const int c0 = 64 * ww + 16 * (tau >> 1) + 4 * gg + 2 * (tau & 1), c1 = 256 + 8 * ww + 2 * gg;
        d[u] = ((tau < 8 ? c0 : c1) + (dd & 1)) * 8192 + (lane & 7) * 16;
    }
    if (lane >= 32) d[4] = 0x40000000u;
    asm volatile("" : "+v"(d[0]), "+v"(d[1]), "+v"(d[2]), "+v"(d[3]), "+v"(d[4]));
}
__device__ __forceinline__ void filter_in(char* lds, u32x4 (&o)[5], int i4, int lane) {
    const char* la = lds + L_W + i4 * 6144 + (lane & 7) * 80 + (lane >> 3) * 2 + ((lane & 1) ^ 1) * 2;
#pragma unroll
    for (int u = 0; u < 5; ++u)
#pragma unroll
        for (int j = 0; j < 4; ++j) o[u][j] = min(((unsigned)*(const LAS u16*)(la + j * 640 + u * 16 + 3072) << 16) | *(const LAS u16*)(la + j * 640 + u * 16), MK);
}
__device__ __forceinline__ void warm_flags(char* ws, int bx, int i4, int lane) {
    unsigned kf = (unsigned)(O_KF - O_FLG) + ((bx & 3) * 8 + (bx >> 5)) * 8192 + ((bx & 4) + ((bx >> 3) & 3)) * 128 + i4 * 2048;
    unsigned tf = i4 < 2 && bx < 144 ? F_RDY + (256 + i4 * 256 + bx) * 128 : 0x40000000;
    asm volatile("" : "+s"(kf), "+s"(tf));
    __builtin_amdgcn_raw_buffer_store_b128(u32x4{}, mkrsrc(ws + O_FLG, (int)(O_KF - O_FLG) + 524288),
                                           (lane < 32 ? kf + (lane >> 4) * 262144 + (lane & 8) * 128 : tf + (lane & 24) * 0x1000000) + (lane & 7) * 16, 0, 16);
}
__device__ __forceinline__ u32x4 neg8(u32x4 v) { return v ^ 0x80008000u; }
struct FC { u32x4 f64r[2], f64i[2], n64i[2], g64r[2], g64i[2], m64i[2], f32r[2], f32i[2], n32i[2]; };
template <class H> __device__ __forceinline__ void fc_make(FC& K, int w, int lane, H&& between) {
    const int col = lane & 15;
    const float g4 = (float)(4 * (lane >> 4));
    float c[4], s[4];
    const unsigned odd = (col & 1) ? 0x80008000u : 0u;
    auto dft64 = [&](int k, float sc, u32x4 (&fr)[2], u32x4 (&fi)[2]) {
#pragma unroll
        for (int i = 0; i < 4; ++i) { const float a = (g4 + (float)i) * ((float)(16 * w + k) * (1.f / 64.f)); c[i] = sc * __builtin_amdgcn_cosf(a); s[i] = -sc * __builtin_amdgcn_sinf(a); }
        const unsigned r0 = pkh(c[0], c[1]), r1 = pkh(c[2], c[3]), i0 = pkh(s[0], s[1]), i1 = pkh(s[2], s[3]);
        const unsigned sr = (k & 2) ? 0x80008000u : 0u, si = sr ^ odd;
        fr[0] = u32x4{r0, r1, ((col & 1) ? i0 : r0) ^ sr, ((col & 1) ? i1 : r1) ^ sr};
        fi[0] = u32x4{i0, i1, ((col & 1) ? r0 : i0) ^ si, ((col & 1) ? r1 : i1) ^ si};
        fr[1] = fr[0] ^ odd; fi[1] = fi[0] ^ odd;
    };
    dft64(col, 1.f, K.f64r, K.f64i);
    between(K.f64r, K.f64i, 1);
    dft64(unmu(col), 1.f / 32.f, K.g64r, K.g64i);
    between(K.g64r, K.g64i, 2);
#pragma unroll
    for (int i = 0; i < 4; ++i) { const float a = (g4 + (float)i) * ((float)col * (1.f / 32.f)); c[i] = __builtin_amdgcn_cosf(a); s[i] = -__builtin_amdgcn_sinf(a); }
    const unsigned r0 = pkh(c[0], c[1]), r1 = pkh(c[2], c[3]), i0 = pkh(s[0], s[1]), i1 = pkh(s[2], s[3]);
    K.f32r[0] = u32x4{r0, r1, r0 ^ odd, r1 ^ odd}; K.f32i[0] = u32x4{i0, i1, i0 ^ odd, i1 ^ odd};
    K.f32r[1] = K.f32r[0] ^ 0x80000000u; K.f32i[1] = K.f32i[0] ^ 0x80000000u;
#pragma unroll
    for (int i = 0; i < 2; ++i) {
        K.n64i[i] = neg8(K.f64i[i]); K.m64i[i] = neg8(K.g64i[i]); K.n32i[i] = neg8(K.f32i[i]);
        asm volatile("" : "+v"(K.f64r[i]), "+v"(K.f64i[i]), "+v"(K.n64i[i]), "+v"(K.g64r[i]), "+v"(K.g64i[i]), "+v"(K.m64i[i]), "+v"(K.f32r[i]), "+v"(K.f32i[i]), "+v"(K.n32i[i]));
    }
}
__device__ __forceinline__ u32x2 prepare(const u16* __restrict__ x, const u16* __restrict__ wr, const u16* __restrict__ wi, const u16* __restrict__ wqkv,
                                         const u16* __restrict__ wo, char* __restrict__ ws, char* lds, int bx, int w, int lane, FC& K) {
    unsigned pf, d[5];
    {
        const char* pc; int len;
        asm volatile("s_getpc_b64 %0\n1:\n s_mov_b32 %1, text_end_mark - 1b" : "=s"(pc), "=s"(len));
        if (bx < 16) asm volatile("buffer_load_dword %0, %1, %2, 0 offen" : "=v"(pf) : "v"(((bx >> 3) + 2 * threadIdx.x) * 128), "s"(mkrsrc(pc, len)));
    }
    if (w == 5) *(LAS u32x4*)(lds + L_CNT + (lane & 16)) = u32x4{};
    unsigned long long ep;
    asm volatile("s_waitcnt lgkmcnt(0)\n s_load_dwordx2 %0, %6, %7" : "=&s"(ep), "+s"(x), "+s"(wr), "+s"(wi), "+s"(wqkv), "+s"(wo) : "s"(ws), "s"((unsigned)(O_FLG + F_EP)) : "memory");
    const unsigned lb = (unsigned)(uintptr_t)lds;
    const int i4 = w & 3;
    const bool mid = (unsigned)(bx - 16) < 128u, isx = mid && w < 4, isq = bx >= 144 && w < 4, iso = bx >= 144 && bx < 180 && w >= 4;
    const int vb = mid ? bx - 16 : isq ? 14 * (bx & 7) + ((bx - 144) >> 3) : bx - 144;
    u32x3 raw[6];
    auto rows = [&](const u16* p, int bytes, int row) {
        const int off = row * 1152 + 48 * (lane >> 4) + 12 * i4;
#pragma unroll
        for (int e = 0; e < 6; ++e) asm volatile("buffer_load_dwordx3 %0, %1, %2, 0 offen offset:%3" : "=v"(raw[e]) : "v"(off), "s"(mkrsrc(p, bytes)), "n"(192 * e));
    };
    if (isx) {
#pragma unroll
        for (int m = 0; m < 5; ++m) {
            const int lo = m ? m - 1 : 0, hi = m < 4 ? m : 3, o = lane * 16 + m * 1024 + (lane < 9 * m ? (lo + (lo & 2)) * 36864 - lo * 1168 : (hi + (hi & 2)) * 36864 - hi * 1168);
            if (m < 4 || lane < 36)
                asm volatile("s_mov_b32 m0, %0\n\ts_nop 0\n\tbuffer_load_dwordx4 %1, %2, %3 offen lds"
                             :: "s"(lb + L_F2 + 24576 + w * 4672 + m * 1024), "v"(o), "s"(mkrsrc(x, S * D * 2)), "s"(nrow(vb * 16 + 4 * w) * 1152) : "memory");
        }
    }
    SB();
    auto marks = [&]() {
        const u32x4 mk = {~0u, ~0u, ~0u, ~0u};
        const unsigned o = (lane >> 3) * 16384 + vb * 128 + (lane & 7) * 16;
#pragma unroll
        for (int u = 0; u < 4; ++u) st16(ws, O_XH0 + (36 * i4 + 8 * u) * 16384, o, mk);
        st16(ws, O_XH0, lane < 32 ? o + (36 * i4 + 32) * 16384 : lane == 32 ? 144 * 16384 + vb * 64 + i4 * 16 : lane >= 56 && i4 == 0 ? (unsigned)(O_FLG + F_RDY) + bx * 128 + (lane & 7) * 16 : 0x40000000, mk);
#pragma unroll
        for (int u = 0; u < 5; ++u) st16(ws, filter_base(vb), d[u], mk);
    };
    if (mid && w >= 4) filter_where(d, vb, i4, lane);
    SB();
    if (w >= 4 || bx < 16) asm volatile("s_barrier" ::: "memory");
    if (mid && w >= 4) {
        marks();
        const rsrc_t rr = mkrsrc(wr, S * 289 * 2), ri = mkrsrc(wi, S * 289 * 2);
#pragma unroll
        for (int m = 0; m < 3; ++m) {
            const int L = 64 * m + lane, row = (L * 205) >> 10;
            const unsigned o = L < 160 ? L * 16 + (row >> 3) * (64 * 578 - 640) + (row & 7) * 498 + (row & 1) * 2 : 0x40000000u;
            asm volatile("s_mov_b32 m0, %0\n\ts_nop 0\n\tbuffer_load_dwordx4 %2, %3, %5 offen lds\n\ts_mov_b32 m0, %1\n\ts_nop 0\n\tbuffer_load_dwordx4 %2, %4, %5 offen lds"
                         :: "s"(lb + L_W + i4 * 6144 + m * 1024), "s"(lb + L_W + i4 * 6144 + 3072 + m * 1024), "v"(o), "s"(rr), "s"(ri),
                            "s"(((vb & 12) * 4 + (vb & 1) * 8 + (vb >> 4) * 256) * 578 + (vb & 2) * 144 + i4 * 72) : "memory");
        }
    }
    SB();
    if (isq) {
        const rsrc_t rs = mkrsrc(wqkv, vb < 108 ? D * 3 * D * 2 : 0);
        const int i = (lane >> 1) & 3, o = 24 * ((lane >> 3) & 3) * 3456 + (lane & 1) * 16 + vb * 32;
        const int o0 = o + (96 * (i + (i >> 1)) + (lane >> 5)) * 3456, o1 = o + (192 + 288 * (i & 1) + 2 * (lane >> 5) + (i >> 1)) * 3456;
#pragma unroll
        for (int k = 0; k < 5; ++k) {
            const int m = k < 4 ? (k + vb) & 3 : 4;
            asm volatile("s_mov_b32 m0, %0\n\ts_nop 0\n\tbuffer_load_dwordx4 %1, %2, %3 offen lds"
                         :: "s"(lb + L_F2 + 24576 + w * 5120 + m * 1024), "v"(m < 3 ? o0 : o1), "s"(rs), "s"((6 * w + (m < 3 ? 2 * m : 4 * m - 12)) * 3456) : "memory");
        }
    }
    SB();
    if (w >= 4) {
        if (w >= 6) { const unsigned v = (lane & 1) ? 0u : 0x3F803F80u; *(LAS u32x4*)(lds + ATT_CONST + (w * 4 - 24 + (lane >> 4)) * 1280 + ((lane >> 2) & 3) * 288 + (2 + (lane & 3)) * 16) = u32x4{v, v, v, v}; }
        if (!mid) warm_flags(ws, bx, i4, lane);
        if (bx >= 184) {
#pragma unroll
            for (int i = 0; i < 8; ++i) st16(ws, O_XH1 + (bx - 184) * 32768 + i4 * 8192 + (i >> 2) * 4096, lane * 16 + (i & 3) * 1024, u32x4{~0u, ~0u, ~0u, ~0u});
        }
        if (bx >= 192) {
            st4(ws, O_SS + (bx - 192) * 1024 + i4 * 256, lane * 4, ~0u);
            st4(ws, O_FLG + (bx - 192) * 2048 + i4 * 512, lane < 4 ? lane * 128 : 0x40000000, 0u);
        }
        if (iso && bx >= 16) __builtin_amdgcn_s_sleep(WOD);
    }
    if (iso) rows(wo, D * D * 2, ofeat(2 * vb + ((lane >> 3) & 1), lane & 7));
    SB();
    u32x3 t2[6], t0[6];
    unsigned c;
    u32x2 dq;
    if (isx || isq || iso) c = fft576a(mid ? 0 : isq ? 1 : 2, ws, lds + (w < 4 ? L_F2 : 0), vb, i4, lane, raw, t2, t0, dq, lb + L_CNT + 16 + (w & 4), ep);
    SB();
    if (mid && w >= 4) {
        await(lb + L_CNT, 4u);
        __builtin_amdgcn_s_setprio(3);
        const char* a = lds + L_F2 + 24576 + (4 * i4 + (lane >> 4)) * 1168 + (lane & 15) * 8;
        float m = 0.f, q = 0.f;
#pragma unroll
        for (int j = 0; j < 9; ++j) { const u32x2 t = *(const LAS u32x2*)(a + j * 128); m = sum2(t[1], sum2(t[0], m)); q = sq2(t[1], sq2(t[0], q)); }
        m = sum16(m); q = sum16(q);
        q = fmaxf(q - m * m * (1.f / 576.f), 0.f) * (4096.f / 576.f) + 0.04096f;
        m = __builtin_amdgcn_rsqf(q);
        *(LAS float*)(lds + L_F2 + 43264 + i4 * 16 + (lane >> 4) * 4) = m;
        asm volatile("s_waitcnt vmcnt(6) lgkmcnt(0)" : "+s"(ep) :: "memory");
        const unsigned now = (unsigned)__builtin_amdgcn_s_memrealtime();
        post(ws, bx, arrive(lb + L_CNT + 16) == 7u ? lane : 64, mktag(ep), now);
        st4(ws, O_XH0 + 144 * 16384 + vb * 64 + i4 * 16, (lane & 15) ? 0x40000000 : lane >> 2, min(__float_as_uint(q * m), MK));
        __builtin_amdgcn_s_setprio(0);
        asm volatile("s_waitcnt vmcnt(2)" ::: "memory");
        u32x4 o[5];
        filter_in(lds, o, i4, lane);
#pragma unroll
        for (int u = 0; u < 5; ++u) st16(ws, filter_base(vb), d[u], o[u]);
    }
    if (bx < 144 && (w < 4 || bx < 16)) fc_make(K, i4, lane, [](u32x4 (&)[2], u32x4 (&)[2], int) {});
    SB();
    if (isx || isq || iso) fft576b(mid ? 0 : isq ? 1 : 2, ws, lds + (w < 4 ? L_F2 : 0), vb, i4, lane, t2, t0, dq, lb + L_CNT + 16 + (w & 4), c);
    if (isx) warm_flags(ws, bx, i4, lane);
    if (bx < 16) asm volatile("s_waitcnt vmcnt(0)" :: "v"(pf));
    asm volatile("s_waitcnt lgkmcnt(0)" : "+s"(ep));
    return mktag(ep);
}

constexpr int G_C0 = 300, G_CX = 130, G_T0 = 200, G_T1 = 200, G_C1 = 215, G_C2 = 200, G_SL0 = 35, G_SL1 = 35, G_Q = 230;
constexpr int F2D = 40, PSL = 3, SPD = 40, SP0 = 20, EF = 6, WY = 0x500800;
__device__ __forceinline__ void gate(unsigned when) {
#pragma nounroll
    for (int spin = 0; spin < 0x4000; ++spin) {
        if ((__builtin_amdgcn_ballot_w64((int)((unsigned)__builtin_amdgcn_s_memrealtime() - when) < 0) >> 32) == 0) break;
        __builtin_amdgcn_s_sleep(1);
    }
}
__device__ __forceinline__ void look(u32x3& v, rsrc_t r, unsigned off) { asm volatile("buffer_load_dwordx3 %0, %1, %2, 0 offen sc1" : "=v"(v) : "v"(off), "s"(r) : "memory"); }
template <int PART> __device__ __forceinline__ bool missing(u32x3 v, u32x2 tag) {
    const unsigned long long no = __builtin_amdgcn_ballot_w64(v[0] != tag[0] || v[1] != tag[1]);
    return (PART == 1 ? (unsigned)no : PART ? no >> 32 : no) != 0;
}
template <int PART> __device__ __forceinline__ void poll(u32x3& v, rsrc_t r, unsigned off, u32x2 tag) {
#pragma nounroll
    for (int spin = 0; spin < 0x100000; ++spin) {
        const bool ok = v[0] == tag[0] && v[1] == tag[1];
        if (__builtin_expect(!missing<PART>(v, tag), 1)) break;
        __builtin_amdgcn_s_sleep(PSL);
        if (!ok) asm volatile("buffer_load_dwordx3 %0, %1, %2, 0 offen sc1\n s_waitcnt vmcnt(0)" : "+v"(v) : "v"(off), "s"(r) : "memory");
    }
}
__device__ __forceinline__ void stage_w(int q, rsrc_t rq, rsrc_t rf, unsigned lb, int w, unsigned l16) {
    asm volatile("s_mov_b32 m0, %0\n\ts_nop 0\n\tbuffer_load_dwordx4 %1, %2, %3 offen sc1 lds"
                 :: "s"(lb + L_W + w * 1024 + q * 8192), "v"(l16), "s"(q < 8 ? rq : rf), "s"(w * 1024 + (q & 7) * 8192) : "memory");
}
template <bool LAST>
__device__ __forceinline__ unsigned fft_phase(char* ws, char* lds0, int bx, int w8, int lane, unsigned fl, u32x2 tag, const u16* wo, FC& K) {
    char* lds = lds0;
    const unsigned lb = (unsigned)(uintptr_t)lds0;
    const int w = w8 & 3, z = w8 >> 2, col = lane & 15, g = lane >> 4, h = (bx & 3) * 8 + (bx >> 5);
    constexpr int NW = LAST ? 13 : 8;
    const rsrc_t rq = mkrsrc(ws + O_WQH + (size_t)h * HIMG, (int)HIMG), rf = mkrsrc((const char*)&F2H + (size_t)(h >> 1) * 36864, 36864);
    const rsrc_t rg = mkrsrc(ws + O_FLG + F_RDY, 98304);
    if (LAST) { tag = *(const LAS u32x2*)(lds0 + L_CNT + 24); tag[0] = __builtin_amdgcn_readfirstlane(tag[0]); tag[1] = __builtin_amdgcn_readfirstlane(tag[1]); }
    const int un = 18 * min(lane & 6, 4) + ((9 * h) >> 3) + (lane & 1);
    const unsigned pa = (16 + 32 * w + (lane & 31)) * 128;
    const unsigned pc = (lane >= 32 ? 256 + 152 + 18 * w + min(lane, 49) : 144 + 8 * un - 111 * ((un * 147) >> 11)) * 128;
    const unsigned pt = ((LAST ? 512 : 256) + 18 * w8 + min(lane & 31, 17)) * 128;
    u32x3 fa, fq, ft;
    if (bx >= 144) {
        if (LAST) {
            __builtin_amdgcn_s_sleep(8);
#pragma unroll
            for (int q = 0; q < NW; ++q) stage_w(q, rq, rf, lb, w8, lane * 16);
            const int id = (bx - 144) * 8 + w8, kb = id / 9, n = (id - kb * 9) * 64 + lane;
            if (kb < 72) {
                u32x4 o;
                const rsrc_t rs = mkrsrc(wo, D * D * 2);
#pragma unroll
                for (int i = 0; i < 4; ++i)
                    o[i] = __builtin_amdgcn_raw_buffer_load_b16(rs, n * 2, ofeat(kb, 2 * i) * D * 2, 0) | ((unsigned)__builtin_amdgcn_raw_buffer_load_b16(rs, n * 2, ofeat(kb, 2 * i + 1) * D * 2, 0) << 16);
                st16(ws, O_WO2, gimg_off(kb, n), o);
            }
            if (bx == 255 && w8 == 7) {
                const u32x2 e = __builtin_amdgcn_raw_buffer_load_b64(mkrsrc(ws + O_FLG + F_EP, 8), 0, 0, 16);
                st8(ws, O_FLG + F_EP, lane ? 0x40000000 : 0, mktag(__builtin_bit_cast(unsigned long long, e)));
            }
            gate(fl + G_C1 + G_C2);
        } else if (!z || bx >= 180) {
            done(ws, lb, bx, w8, lane, tag);
            if (z && bx >= 192) {
                __builtin_amdgcn_s_sleep(F2D);
                touch((const char*)&F2H + ((bx & 3) * 8 + ((bx - 192) >> 3)) * 18432, 18432, w * 64 + lane, 256);
            }
            if (z && bx < 192) __builtin_amdgcn_s_sleep(SPD);
            __builtin_amdgcn_s_sleep(SP0);
            look(fa, rg, pa);
            asm volatile("s_waitcnt vmcnt(0)" : "+v"(fa));
            poll<0>(fa, rg, pa, tag);
            gate(fa[2] + G_Q);
            look(fq, rg, pc);
            asm volatile("s_waitcnt vmcnt(0)" : "+v"(fq));
            poll<1>(fq, rg, pc, tag);
#pragma unroll
            for (int q = 0; q < NW; ++q) stage_w(q, rq, rf, lb, w8, lane * 16);
            if (bx < 180) {
#pragma unroll
                for (int q = 0; q < NW; ++q) stage_w(q, rq, rf, lb, w8 + 4, lane * 16);
            }
            poll<0>(fq, rg, pc, tag);
            poll<0>(fa, rg, pa, tag);
            gate(fa[2] + G_C0);
        }
        look(ft, rg, pt);
        asm volatile("s_waitcnt vmcnt(0)" : "+v"(ft) :: "memory");
        poll<0>(ft, rg, pt, tag);
        return ft[2];
    }
    const int kb = 8 * (bx >> 4) + (bx & 7), half = (bx >> 3) & 1;
    const bool dc = bx == 0;
    const rsrc_t ri = mkrsrc(ws + (LAST ? O_XH2 : O_XH0), (int)PLANES);
    const int pl = (2 * kb + half) * 16384;
    int xo = (4 * w8 + g) * 512 + (col >> 2) * 128 + (col & 3) * 16;
    u32x4 xa, xb, w0, w1;
    u32x2 sp[8];
    unsigned sg[4];
    auto mx = [](u32x4 a, u32x4 b) { return max(max(max(a[0], a[1]), max(a[2], a[3])), max(max(b[0], b[1]), max(b[2], b[3]))); };
    auto fresh = [&]() {
        asm volatile("" : "=v"(xa), "=v"(xb), "=v"(w0), "=v"(w1));
#pragma unroll
        for (int i = 0; i < 8; ++i) { if (LAST) asm volatile("" : "=v"(sp[i])); else if (i < 4) asm volatile("" : "=v"(sg[i])); }
    };
    auto fetch = [&]() {
        asm volatile("buffer_load_dwordx4 %0, %2, %3, %4 offen sc1\n buffer_load_dwordx4 %1, %2, %3, %4 offen offset:64 sc1" : "+v"(xa), "+v"(xb) : "v"(xo), "s"(ri), "s"(pl));
#pragma unroll
        for (int i = 0; i < (LAST ? 8 : 0); ++i)
            asm volatile("buffer_load_dwordx2 %0, %1, %2, %3 offen offset:%4 sc1" : "+v"(sp[i]) : "v"(lane * 8), "s"(ri), "s"((144 + (i >> 2)) * 16384 + w8 * 2048), "n"((i & 3) * 512));
    };
    auto filter = [&]() {
        asm volatile("buffer_load_dwordx4 %0, %2, %3, %4 offen sc1\n buffer_load_dwordx4 %1, %2, %3, %5 offen sc1" : "+v"(w0), "+v"(w1)
                     : "v"(lane * 16), "s"(mkrsrc(ws + O_WT + (4 * kb + 2 * half + z) * 8192, 8192)), "s"(w * 1024), "s"(w * 1024 + 4096));
#pragma unroll
        for (int i = 0; i < (LAST ? 0 : 4); ++i)
            asm volatile("buffer_load_dword %0, %1, %2, %3 offen offset:%4 sc1" : "+v"(sg[i]) : "v"(g * 1024 + col * 4), "s"(ri), "s"(144 * 16384 + z * 4096 + w * 64), "n"(i * 256));
    };
    constexpr int WP = LAST ? WY : 0x800000, N0 = (WP >> 16) & 15, N1 = N0 + ((WP >> 12) & 15), N2 = N1 + ((WP >> 8) & 15), N3 = N2 + ((WP >> 4) & 15), N4 = N3 + (WP & 15), N5 = NW - (WP >> 20);
    auto stage = [&](int q0, int q1) {
        if (q1 > q0) SB();
#pragma unroll
        for (int q = q0; q < q1; ++q) stage_w(q, rq, rf, lb, w8, lane * 16);
        if (q1 > q0) SB();
    };
    stage(0, N0);
    bool asked = false, todo = !LAST && z && bx >= 16;
    auto maybe = [&]() {
        SB();
        if (!asked && (__builtin_amdgcn_ballot_w64((int)((unsigned)__builtin_amdgcn_s_memrealtime() - fl) < G_C1) >> 32) == 0) { fetch(); asked = true; }
        SB();
    };
    if (LAST) { fresh(); filter(); }
    if (LAST) fc_make(K, w, lane, [&](u32x4 (&a)[2], u32x4 (&b)[2], int i) {
        if ((EF >> i) & 1) { asm volatile("" : "+v"(a[0]), "+v"(a[1]), "+v"(b[0]), "+v"(b[1])); maybe(); }
    });
    auto& f64r = K.f64r; auto& f64i = K.f64i; auto& n64i = K.n64i; auto& g64r = K.g64r; auto& g64i = K.g64i; auto& m64i = K.m64i; auto& f32r = K.f32r; auto& f32i = K.f32i; auto& n32i = K.n32i;
    float t1c[8], t1s[8], t2c[8], t2s[8];
    const float g4 = (float)(4 * g), k1 = (float)(16 * w + col) * (1.f / 2048.f), fc = (float)col * (1.f / 2048.f);
    const unsigned one = (col >> 2) == g ? ((col & 1) ? 0x3F800000u : 0x3F80u) : 0u;
    u32x4 sel = {(col & 2) ? 0u : one, (col & 2) ? one : 0u, (col & 2) ? 0u : one, (col & 2) ? one : 0u};
    unsigned xi = L_FFT + z * 2048 + (col >> 3) * 4096 + (col & 3) * 256 + ((4 * w + g) ^ (col & 3) ^ ((col >> 1) & 4)) * 16 + ((col >> 2) & 1) * 8;
    unsigned xr0 = L_FFT + z * 8192 + g * 256 + (col ^ g) * 16, xr1 = xr0 ^ 64, ro = (16 * z + 4 * g) * 512 + (16 * w + col) * 8, rp = ro + (16 * w + col) * 8;
    if (LAST) asm volatile("" : "+v"(sel));
    asm volatile("" : "+v"(xi), "+v"(xr0), "+v"(xr1), "+v"(ro), "+v"(rp), "+v"(xo));
    SB();
    auto twiddles = [&]() {
        const float a0 = g4 * k1, k4 = g4 + (float)(16 * w), fd = fc + 16.f / 2048.f, b0 = k4 * fc, b1 = k4 * fd;
#pragma unroll
        for (int i = 0; i < 8; ++i) {
            const float a = __builtin_fmaf((float)(16 * (i >> 2) + (i & 3)), k1, a0);
            t1c[i] = __builtin_amdgcn_cosf(a); t1s[i] = __builtin_amdgcn_sinf(a);
            const float b = __builtin_fmaf((float)(i & 3), i < 4 ? fc : fd, i < 4 ? b0 : b1);
            t2c[i] = __builtin_amdgcn_cosf(b); t2s[i] = __builtin_amdgcn_sinf(b);
            asm volatile("" : "+v"(t1c[i]), "+v"(t1s[i]), "+v"(t2c[i]), "+v"(t2s[i]));
            SB();
        }
    };
    if (!LAST) fresh();
    if (LAST) { if (!asked) gate(fl + G_C1); }
    else {
        asm volatile("" : "=v"(fa));
        if (z) {
            if (bx < 16) __builtin_amdgcn_s_sleep(B16);
            asm volatile("buffer_load_dwordx3 %0, %1, %2, 0 offen sc1" : "+v"(fa) : "v"(pa), "s"(rg) : "memory");
        }
        SB();
        twiddles();
        if (z) {
            asm volatile("s_waitcnt vmcnt(0)" : "+v"(fa));
            if (todo && missing<0>(fa, tag)) { fc_make(K, w, lane, [](u32x4 (&)[2], u32x4 (&)[2], int) {}); todo = false; }
            poll<0>(fa, rg, pa, tag);
            gate(fa[2] + G_CX);
        }
        asm volatile("s_barrier" ::: "memory");
    }
    if (!asked) fetch();
    SB();
    if (LAST) twiddles();
    if (todo) fc_make(K, w, lane, [](u32x4 (&)[2], u32x4 (&)[2], int) {});
    stage(N0, N1);
    if (LAST) asm volatile("s_waitcnt vmcnt(%2)" : "+v"(w0), "+v"(w1) : "n"(N1 - N0));
    else asm volatile("s_waitcnt vmcnt(0)");
    f32x4 sq;
    u32x4 ss[4];
    auto sums = [&]() {
        asm volatile("" : "+v"(xa), "+v"(xb));
        if (!LAST) return false;
#pragma unroll
        for (int i = 0; i < 8; ++i) asm volatile("" : "+v"(sp[i]));
#pragma unroll
        for (int i = 0; i < 4; ++i) ss[i] = u32x4{sp[2 * i][0], sp[2 * i][1], sp[2 * i + 1][0], sp[2 * i + 1][1]};
        sq = MF16(asb(sel), asb(ss[1]), MF16(asb(sel), asb(ss[0]), f32x4{})) + MF16(asb(sel), asb(ss[3]), MF16(asb(sel), asb(ss[2]), f32x4{}));
        return sq[0] != sq[0];
    };
    if (const bool nn = sums(); __builtin_expect(__any(mx(xa, xb) == ~0u || nn), 0)) {
#pragma nounroll
        for (int spin = 0; spin < 0x40000; ++spin) {
            if (!__any(max(mx(xa, xb), LAST ? max(mx(ss[0], ss[1]), mx(ss[2], ss[3])) : 0u) == ~0u)) break;
            fetch();
            asm volatile("s_waitcnt vmcnt(0)");
            sums();
        }
    }
    {
        float rho[4] = {};
#pragma unroll
        for (int i = 0; i < (LAST ? 4 : 0); ++i) rho[i] = __builtin_amdgcn_rsqf(sq[i] * (4096.f / 576.f) + 0.04096f);
#pragma unroll
        for (int c = 0; c < 2; ++c) {
            u32x2 re = {pkh(blo(xa[c]) * rho[0], blo(xa[2 + c]) * rho[1]), pkh(blo(xb[c]) * rho[2], blo(xb[2 + c]) * rho[3])};
            u32x2 im = {pkh(bhi(xa[c]) * rho[0], bhi(xa[2 + c]) * rho[1]), pkh(bhi(xb[c]) * rho[2], bhi(xb[2 + c]) * rho[3])};
            if (!LAST) {
                re = u32x2{__builtin_amdgcn_perm(xa[2 + c], xa[c], 0x05040100u), __builtin_amdgcn_perm(xb[2 + c], xb[c], 0x05040100u)};
                im = u32x2{__builtin_amdgcn_perm(xa[2 + c], xa[c], 0x07060302u), __builtin_amdgcn_perm(xb[2 + c], xb[c], 0x07060302u)};
            }
            if (c == 0 && dc) { re = im; im = u32x2{0u, 0u}; }
            *(LAS u32x2*)(lds0 + xi + c * 8192) = re; *(LAS u32x2*)(lds0 + xi + c * 8192 + 1024) = im;
        }
    }
    float wc[8], wd[8];
    auto unpack = [&]() {
#pragma unroll
        for (int i = 0; i < 8; ++i) { const unsigned wv = i < 4 ? w0[i & 3] : w1[i & 3]; wc[i] = blo(wv); wd[i] = bhi(wv); asm volatile("" : "+v"(wc[i]), "+v"(wd[i])); }
    };
    if (LAST) unpack();
    else { SB(); filter(); SB(); }
    asm volatile("s_waitcnt lgkmcnt(0)\n\ts_barrier" ::: "memory");
    post(ws, (LAST ? 512 : 256) + bx, w8 ? 64 : lane, tag, (unsigned)__builtin_amdgcn_s_memrealtime() + (LAST ? G_T1 : G_T0));
    char* xw = lds + (w >> 1) * 4096 + lane * 16 + (w & 1) * 8;
    const char* xr = lds + lane * 16;
    f32x4 yr[2] = {}, yi[2] = {};
    u32x4 ar[4], ai[4];
#pragma unroll
    for (int q = 0; q < 4; ++q) { const char* a = lds0 + ((q & 1) ? xr1 + 4096 : xr0) + (q >> 1) * 2048; ar[q] = lds128(a); ai[q] = lds128(a + 1024); }
    u32x2 xs[4];
#pragma unroll
    for (int i = 0; i < 4; ++i) asm volatile("buffer_load_dwordx2 %0, %1, %2, %3 offen offset:%4 sc1" : "=v"(xs[i]) : "v"(ro), "s"(ri), "s"(pl), "n"(i * 512));
    if (!LAST) look(fq, rg, pc);
    SB();
    stage(N1, N2);
#pragma unroll
    for (int t = 0; t < 2; ++t)
#pragma unroll
        for (int s = 0; s < 2; ++s) {
            yr[t] = MH16(ash(ar[2 * t + s]), ash(f64r[s]), yr[t]); yi[t] = MH16(ash(ar[2 * t + s]), ash(f64i[s]), yi[t]);
            yr[t] = MH16(ash(ai[2 * t + s]), ash(n64i[s]), yr[t]); yi[t] = MH16(ash(ai[2 * t + s]), ash(f64r[s]), yi[t]);
        }
    stage(N2, N3);
    u32x4 br, bi;
    {
        float vr[8], vi[8];
#pragma unroll
        for (int i = 0; i < 8; ++i) { const float a = yr[i >> 2][i & 3], b = yi[i >> 2][i & 3]; vr[i] = a * t1c[i] + b * t1s[i]; vi[i] = b * t1c[i] - a * t1s[i]; }
#pragma unroll
        for (int q = 0; q < 4; ++q) { br[q] = pkh(vr[2 * q], vr[2 * q + 1]); bi[q] = pkh(vi[2 * q], vi[2 * q + 1]); }
    }
#pragma unroll
    for (int t = 0; t < 2; ++t) {
        yr[t] = MH16(ash(f32r[t]), ash(br), f32x4{}); yi[t] = MH16(ash(f32i[t]), ash(br), f32x4{});
        yr[t] = MH16(ash(n32i[t]), ash(bi), yr[t]); yi[t] = MH16(ash(f32r[t]), ash(bi), yi[t]);
    }
    stage(N3, N4);
    if (!LAST) {
        SB();
        asm volatile("s_waitcnt vmcnt(6)" : "+v"(w0), "+v"(w1), "+v"(sg[0]), "+v"(sg[1]), "+v"(sg[2]), "+v"(sg[3]));
        auto bad = [&]() { return __any(max(mx(w0, w1), max(max(sg[0], sg[1]), max(sg[2], sg[3]))) == ~0u); };
        if (__builtin_expect(bad(), 0)) {
#pragma nounroll
            for (int spin = 0; spin < 0x40000; ++spin) {
                filter();
                asm volatile("s_waitcnt vmcnt(0)" : "+v"(w0), "+v"(w1), "+v"(sg[0]), "+v"(sg[1]), "+v"(sg[2]), "+v"(sg[3]));
                if (!bad()) break;
            }
        }
        unpack();
    }
    {
        float vr[8], vi[8];
#pragma unroll
        for (int i = 0; i < 8; ++i) { const float a = yr[i >> 2][i & 3], b = yi[i >> 2][i & 3]; vr[i] = a * wc[i] - b * wd[i]; vi[i] = a * wd[i] + b * wc[i]; }
#pragma unroll
        for (int q = 0; q < 4; ++q) { br[q] = pkh(vr[2 * q], vr[2 * q + 1]); bi[q] = pkh(vi[2 * q], vi[2 * q + 1]); }
    }
#pragma unroll
    for (int t = 0; t < 2; ++t) {
        yr[t] = MH16(ash(br), ash(f32r[t]), f32x4{}); yi[t] = MH16(ash(br), ash(n32i[t]), f32x4{});
        yr[t] = MH16(ash(bi), ash(f32i[t]), yr[t]); yi[t] = MH16(ash(bi), ash(f32r[t]), yi[t]);
    }
    stage(N4, N5);
    {
        float vr[8], vi[8];
#pragma unroll
        for (int i = 0; i < 8; ++i) { const float a = yr[i >> 2][i & 3], b = yi[i >> 2][i & 3]; vr[i] = a * t2c[i] - b * t2s[i]; vi[i] = b * t2c[i] + a * t2s[i]; }
#pragma unroll
        for (int t = 0; t < 2; ++t) {
            *(LAS u32x2*)(xw + z * 8192 + t * 2048) = u32x2{pkh(vr[4 * t], vr[4 * t + 1]), pkh(vr[4 * t + 2], vr[4 * t + 3])};
            *(LAS u32x2*)(xw + z * 8192 + t * 2048 + 1024) = u32x2{pkh(vi[4 * t], vi[4 * t + 1]), pkh(vi[4 * t + 2], vi[4 * t + 3])};
        }
    }
    asm volatile("s_waitcnt vmcnt(%4)" : "+v"(xs[0]), "+v"(xs[1]), "+v"(xs[2]), "+v"(xs[3]) : "n"(N5 - N1));
    if (!LAST) { asm volatile("" : "+v"(fq)); poll<2>(fq, rg, pc, tag); }
    const bool again = !LAST && missing<1>(fq, tag);
    look(ft, rg, pt);
    f32x4 zr[2], zi[2];
#pragma unroll
    for (int c = 0; c < 2; ++c)
#pragma unroll
        for (int i = 0; i < 4; ++i) {
            zr[c][i] = LAST ? blo(xs[i][c]) : hlo(xs[i][c]); zi[c][i] = LAST ? bhi(xs[i][c]) : hhi(xs[i][c]);
            if (!LAST) { zr[c][i] *= __uint_as_float(sg[i]); zi[c][i] *= __uint_as_float(sg[i]); }
        }
    const f32x4 keep = zr[0];
    if (dc) zr[0] = zi[0];
    asm volatile("s_waitcnt lgkmcnt(0)\n\ts_barrier" : "+v"(zr[0]), "+v"(zr[1]), "+v"(zi[0]), "+v"(zi[1]) :: "memory");
    if (again) asm volatile("buffer_load_dwordx3 %0, %1, %2, 0 offen sc1" : "+v"(fq) : "v"(pc), "s"(rg) : "memory");
#pragma unroll
    for (int q = 0; q < 4; ++q) { ar[q] = lds128(xr + q * 4096 + z * 2048); ai[q] = lds128(xr + q * 4096 + z * 2048 + 1024); }
    SB();
#pragma unroll
    for (int c = 0; c < 2; ++c)
#pragma unroll
        for (int s = 0; s < 2; ++s) {
            zr[c] = MH16(ash(ar[2 * c + s]), ash(g64r[s]), zr[c]); zi[c] = MH16(ash(ar[2 * c + s]), ash(m64i[s]), zi[c]);
            zr[c] = MH16(ash(ai[2 * c + s]), ash(g64i[s]), zr[c]); zi[c] = MH16(ash(ai[2 * c + s]), ash(g64r[s]), zi[c]);
        }
    if (dc) { zi[0] = zr[0]; zr[0] = keep; }
#pragma unroll
    for (int i = 0; i < 4; i += 2)
        st16(ws, (LAST ? O_XH3 : O_XH1) + pl + i * 512, rp,
                 u32x4{min(pk(zr[0][i], zi[0][i]), MK), min(pk(zr[1][i], zi[1][i]), MK), min(pk(zr[0][i + 1], zi[0][i + 1]), MK), min(pk(zr[1][i + 1], zi[1][i + 1]), MK)});
    SB();
    const unsigned when = (unsigned)__builtin_amdgcn_s_memrealtime() + (LAST ? G_SL1 : G_SL0);
    if (again) asm volatile("s_waitcnt vmcnt(2)" : "+v"(fq));
    if (!LAST) poll<1>(fq, rg, pc, tag);
    stage(N5, NW);
    asm volatile("s_waitcnt vmcnt(%1)" : "+v"(ft) : "n"(2 + NW - N5));
    poll<0>(ft, rg, pt, tag);
    fl = ft[2];
    asm volatile("v_writelane_b32 %0, %1, 63" : "+v"(fl) : "s"(when));
    return fl;
}

#define EX(x) __builtin_amdgcn_exp2f(x)
__device__ __forceinline__ float xhalf_max(float m) {
    auto rr = __builtin_amdgcn_permlane32_swap(__float_as_uint(m), __float_as_uint(m), false, false);
    return fmaxf(__uint_as_float(rr[0]), __uint_as_float(rr[1]));
}
__device__ __forceinline__ float max16(const f32x16& s) {
    float a = fmaxf(fmaxf(s[0], s[1]), s[2]), b = fmaxf(fmaxf(s[3], s[4]), s[5]);
    a = fmaxf(fmaxf(a, s[6]), s[7]); b = fmaxf(fmaxf(b, s[8]), s[9]); a = fmaxf(fmaxf(a, s[10]), s[11]); b = fmaxf(fmaxf(b, s[12]), s[13]);
    a = fmaxf(fmaxf(a, s[14]), s[15]);
    return xhalf_max(fmaxf(a, b));
}
#define T_STR2(X) #X
#define T_STR(X) T_STR2(X)
#define A_XP(S, D) "v_exp_f32 v[" D "], v[" S "]\n v_exp_f32 v[" D "+1], v[" S "+1]\n v_exp_f32 v[" D "+2], v[" S "+2]\n v_exp_f32 v[" D "+3], v[" S "+3]\n"
#define A_QK1(SN, KC) "v_mfma_f32_32x32x16_bf16 v[" SN ":" SN "+15], v[" KC ":" KC "+3], v[76:79], 0\n"
#define A_QK2(SN, K2, QN) "v_mfma_f32_32x32x8bf16_1k v[" SN ":" SN "+15], v[" K2 ":" K2 "+1], v[" QN ":" QN "+1], v[" SN ":" SN "+15]\n"
#define A_PV(V, P) "v_smfmac_f32_32x32x32_bf16 v[0:15], v[" V ":" V "+3], v[" P ":" P "+7], v84\n"
#define A_R128(D, A, O) "ds_read_b128 v[" D ":" D "+3], " A " offset:" O "\n"
#define A_R32(D, O) "ds_read_b32 v" D ", v86 offset:" O "\n"
#define A_STEP(SC, Q1, Q2, LD) Q1 A_XP(SC, SC) Q2 A_XP(SC "+4", SC "+4") "s_waitcnt lgkmcnt(0)\n" A_PV("72", "48") A_XP(SC "+8", "48") A_PV("68", SC) LD A_XP(SC "+12", "52")
#define A_VV(T) A_R128("72", "v87", #T "*1280+576") A_R128("68", "v87", #T "*1280+1280")
#define A_PR(N) "s_setprio " #N "\n"
#define A_PRI(N) "s_setprio (" T_STR(PRA) ">>(28-4*" #N "))&3\n"
#define A_S0(H) A_STEP("16", A_QK1("32", "60"), A_QK2("32", "64", "82"), A_VV(0) A_R128("60", "v85", "3072") H)
#define A_S1(H) A_STEP("32", A_QK1("16", "56"), A_QK2("16", "66", "80"), A_VV(1) A_R128("56", "v85", "4096") A_R32("64", "5120") H)
#define A_S2(H) A_STEP("16", A_QK1("32", "60"), A_QK2("32", "66", "82"), A_VV(2) A_R128("60", "v85", "5120") H)
#define A_S3(H) A_STEP("32", A_QK1("16", "56"), A_QK2("16", "64", "80"), A_VV(3) A_R128("56", "v85", "6144") A_R32("66", "7680") H)
#define A_S4(H) A_STEP("16", A_QK1("32", "60"), A_QK2("32", "64", "82"), A_VV(4) A_R128("60", "v85", "7168") H)
#define A_S5(H) A_STEP("32", A_QK1("16", "56"), A_QK2("16", "66", "80"), A_VV(5) H)
#define A_S6(H) A_STEP("16", A_QK1("32", "60"), A_QK2("32", "66", "82"), A_VV(6) H)
#define A_NEXT "v_add_u32 v87, v87, v88\n v_add_u32 v85, 0x4800, v85\n v_add_u32 v86, 0x4800, v86\n"
#define A_S567(H5, H6, H7) A_S5(A_R128("56", "v85", "18432") A_R32("64", "18432") H5) A_S6(A_R128("60", "v85", "19456") H6) \
    A_STEP("32", A_QK1("16", "56"), A_QK2("16", "64", "80"), A_R128("72", "v87", "9536") A_NEXT A_R128("68", "v87", "0") A_R128("56", "v85", "2048") A_R32("66", "2560") H7)
#define A_WL(J) "s_mov_b32 %[tmp], (" J ")*1024\n buffer_load_dwordx4 v[90+4*(" J "):93+4*(" J ")], v89, %[rw], %[tmp] offen sc1\n"
#define A_CHUNK(J, H0) A_S0(H0 A_WL(J)) A_S1(A_WL(J "+1")) A_S2(A_WL(J "+2")) A_S3(A_WL(J "+3")) A_S4(A_WL(J "+4")) A_S567(A_WL(J "+5"), A_WL(J "+6"), A_WL(J "+7"))
#define A_PRIME A_R128("56", "v85", "0") A_R32("64", "0") A_R128("60", "v85", "1024") A_R32("66", "2560") A_R128("68", "v87", "0") \
    "s_waitcnt lgkmcnt(3)\n" A_QK1("16", "56") A_QK2("16", "64", "80") A_R128("56", "v85", "2048") "s_waitcnt lgkmcnt(1)\n s_nop 10\n"
#define A_PRIME0 A_R128("56", "v85", "0") A_R32("64", "0") A_R128("60", "v85", "1024") A_R32("66", "2560") A_R128("68", "v87", "0") \
    "v_mov_b32 v72, 0\n v_mov_b32 v73, 0\n v_mov_b32 v65, 0x3f80\n v_mov_b32 v67, 0x3f80\n v_mov_b32 v84, 0xdddddddd\n v_subrev_u32 v89, %[lb], v85\n" \
    "v_mfma_f32_32x32x8bf16_1k v[0:15], v[72:73], v[72:73], 0\n" \
    "s_waitcnt lgkmcnt(3)\n" A_QK1("16", "56") A_QK2("16", "64", "80") A_R128("56", "v85", "2048") "s_waitcnt lgkmcnt(1)\n s_nop 10\n"
#define A_S0F A_QK1("32", "60") A_XP("16", "16") A_QK2("32", "64", "82") A_XP("16+4", "16+4") A_XP("16+8", "48") "s_waitcnt lgkmcnt(0)\n" A_PV("68", "16") \
    A_VV(0) A_R128("60", "v85", "3072") A_XP("16+12", "52")
#define SGA 1
#define LK1 3
#define LK2 5
#define LK3 9
#define PRA 0x33221100
#define A_TELL "s_waitcnt vmcnt(0)\n s_mov_b64 exec, 0x80\n global_store_dwordx2 v90, v[252:253], %[fb] sc1\n s_mov_b64 exec, -1\n"
#define A_SG(N) ".if " #N " == " T_STR(SGA) "\n s_cmp_lt_u32 %[w], 4\n s_cbranch_scc1 20f\n" A_TELL "20:\n .endif\n"
#define A_LOOK "global_load_dwordx2 v[92:93], v90, %[fb] sc1\n"
#define A_SEE "s_waitcnt vmcnt(0)\n v_cmp_eq_u64 vcc, v[92:93], v[252:253]\n s_nop 3\n s_and_b32 s85, vcc_lo, s84\n"
#define A_REQ "a_req %[tmp], %[so], %[rk]\n"
#define A_LK(N) ".if " #N " == " T_STR(LK1) "\n" A_LOOK ".endif\n .if " #N " == " T_STR(LK2) "\n" A_SEE A_REQ A_LOOK ".endif\n" \
    ".if " #N " == " T_STR(LK3) "\n s_bitcmp1_b32 s84, 0\n s_cbranch_scc0 22f\n" A_SEE A_REQ A_LOOK "22:\n .endif\n"
#define X3A 7
#define A_X3(N) ".if %[nx] && " #N " == " T_STR(X3A) "\n s_mov_b32 exec_hi, 0\n buffer_store_dword v208, v251, %[rs], 0 offen sc1\n s_mov_b64 exec, -1\n s_cmp_lt_u32 %[w], 4\n s_cbranch_scc0 24f\n" \
    "s_mov_b32 %[tmp], 0x10000\n buffer_store_dwordx4 v[200:203], v250, %[rs], 0 offen sc1\n buffer_store_dwordx4 v[204:207], v250, %[rs], %[tmp] offen sc1\n 24:\n .endif\n"
#define A_BAR "s_waitcnt lgkmcnt(0)\n s_barrier\n"
#define A_BAR0 "s_waitcnt vmcnt(" T_STR(QLP) ") lgkmcnt(0)\n s_barrier\n"
#define A_PZ(O) "buffer_store_dwordx4 v[250:253], v89, %[ro], %[tmp] offen offset:" #O " sc1\n"
#define A_PZ2 A_PZ(0) A_PZ(1024) "s_add_u32 %[tmp], %[tmp], 0x4000\n"
#define A_PZ3 A_PZ(0) A_PZ(1024) A_PZ(2048) "s_add_u32 %[tmp], %[tmp], 3072\n"
#define A_W3(L, X) "s_cmp_eq_u32 %[w], 3\n s_cbranch_scc0 " L "f\n" X L ":\n"

#define T_GO(S) "s_bitcmp1_b32 %[go], 2*" #S "\n s_cbranch_scc0 8" #S "1f\n s_add_u32 %[t], %[ko], " #S "*0x20000\n" \
    "buffer_load_dwordx4 v[16+4*" #S ":19+4*" #S "], v255, %[ro], %[t] offen sc1\n 8" #S "1:\n"
#define T_L1(I) "s_add_u32 %[t], %[lo], " #I "*0x2000\n buffer_load_dword v[48+" #I "], v85, %[ro], %[t] offen sc1\n"
#define T_X1(S) "v_max_u32 v76, v[16+4*" #S "], v[17+4*" #S "]\n"
#define T_X2(S) "v_max3_u32 v76, v76, v[18+4*" #S "], v[19+4*" #S "]\n"
#define T_X3 "v_cmp_eq_u32 vcc, -1, v76\n"
#define T_M(S, T) "v_mfma_f32_16x16x32_bf16 v[52+4*" #T ":55+4*" #T "], v[90+16*" #S "+4*" #T ":93+16*" #S "+4*" #T "], v[16+4*" #S ":19+4*" #S "], v[52+4*" #T ":55+4*" #T "]\n"
#define T_H(S, D, C) "v_mfma_f32_16x16x32_bf16 " D ", v[234+4*(" #S "/2):237+4*(" #S "/2)], v[16+4*" #S ":19+4*" #S "], " C "\n"
#define T_CHK(S, VM) "7" #S "0:\n s_waitcnt vmcnt(" #VM ")\n" T_X1(S) T_X2(S) T_X3 "s_cbranch_vccz 7" #S "1f\n s_mov_b32 %[go], 1<<(2*" #S ")\n" \
    "s_call_b64 %[ret], 79b\n s_branch 7" #S "0b\n 7" #S "1:\n"
#define T_STEP(S, D, C, N, VM) T_H(S, D, C) T_M(S, 0) T_X1(N) T_M(S, 1) T_X2(N) T_M(S, 2) T_X3 T_M(S, 3) "s_cbranch_vccz 7" #N "1f\n" T_CHK(N, VM)
#define T_MK(A) "v_mov_b64 v[" #A ":" #A "+1], -1\n v_mov_b64 v[" #A "+2:" #A "+3], -1\n"
#define T_UP(D, A) "v_lshlrev_b32 v[" D "], 16, v[" A "]\n v_and_b32 v[" D "+1], 0xffff0000, v[" A "]\n"
#define T_R 3850.0
#define T_D 235

#define QLP 6
#define QL1 6
#define QLA 6
#define QLB 8
#define NCHK 6
#define QLC 8
#define SSA 31
#define STL 0
typedef __attribute__((ext_vector_type(4))) short s16x4;
#define P_MACROS \
    ".macro a_req t, so, rk\n s_ff1_i32_b32 \\t, s85\n s_cmp_lt_i32 \\t, 0\n s_cbranch_scc1 21f\n s_bitset0_b32 s85, \\t\n s_bitset0_b32 s84, \\t\n s_mul_i32 m0, \\t, 0x4800\n s_add_u32 \\t, \\t, s89\n" \
    "s_and_b32 \\t, \\t, 7\n s_mul_i32 \\t, \\t, 0x4800\n s_add_u32 m0, m0, s86\n s_add_u32 \\t, \\t, \\so\n buffer_load_dwordx4 v89, \\rk, \\t offen sc1 lds\n s_add_u32 m0, m0, s87\n" \
    "s_add_u32 \\t, \\t, s87\n buffer_load_dwordx4 v89, \\rk, \\t offen sc1 lds\n buffer_load_dword v91, \\rk, \\t offen offset:1024 sc1 lds\n 21:\n .endm\n" \
    ".macro p_load\n s_mov_b32 s88, p_i*65536\n buffer_load_dwordx4 v[100+4*p_i:103+4*p_i], v92, s[84:87], s88 offen sc1\n" \
    ".if p_i == " T_STR(SSA) "\n buffer_load_dwordx3 v[72:74], v96, s[84:87], 0 offen sc1\n .endif\n .set p_i, p_i+1\n .endm\n" \
    ".macro p_ready t\n .if p_i > " T_STR(SSA) " && (\\t) <= " T_STR(SSA) "\n s_waitcnt vmcnt(p_i-(\\t))\n .else\n s_waitcnt vmcnt(p_i-1-(\\t))\n .endif\n" \
    "v_permlane16_swap_b32 v[100+4*(\\t)], v[102+4*(\\t)]\n v_permlane16_swap_b32 v[101+4*(\\t)], v[103+4*(\\t)]\n .if p_i > 35\n s_nop 0\n .endif\n" \
    ".if (\\t) < " T_STR(NCHK) "\n v_max_u32 v75, v[100+4*(\\t)], v[101+4*(\\t)]\n v_max3_u32 v75, v75, v[102+4*(\\t)], v[103+4*(\\t)]\n v_cmp_eq_u32 vcc, -1, v75\n s_cbranch_vccnz 2f\n .endif\n .endm\n" \
    ".macro p_mm d, a, b\n s_waitcnt lgkmcnt(p_n-1)\n" \
    ".if p_s\n v_mfma_f32_32x32x16_bf16 v[\\d:\\d+15], v[\\a:\\a+3], v[\\b:\\b+3], v[\\d:\\d+15]\n .else\n v_mfma_f32_32x32x16_bf16 v[\\d:\\d+15], v[\\a:\\a+3], v[\\b:\\b+3], 0\n .endif\n .endm\n" \
    ".macro p_w d, a, o\n ds_read_b128 v[\\d+4-p_b:\\d+7-p_b], v\\a offset:(p_s+1)*\\o\n .endm\n" \
    ".macro p_cvt d, a, m, k\n .set p_j, 0\n .rept 4\n v_mul_f32 v48, v[\\a+2*p_j], v[\\m+2*p_j*\\k]\n v_mul_f32 v49, v[\\a+2*p_j+1], v[\\m+(2*p_j+1)*\\k]\n" \
    "v_cvt_pk_bf16_f32 v[\\d+p_j], v48, v49\n .set p_j, p_j+1\n .endr\n .endm\n" \
    ".macro p_rho\n s_mov_b32 s83, 0x40000\n 7:\n v_max3_u32 v75, v72, v73, v74\n v_cmp_eq_u32 vcc, -1, v75\n s_cbranch_vccz 9f\n s_sleep 1\n" \
    "buffer_load_dwordx3 v[72:74], v96, s[84:87], 0 offen sc1\n s_waitcnt vmcnt(0)\n s_sub_u32 s83, s83, 1\n s_cbranch_scc0 7b\n 9:\n" \
    "v_add_f32 v72, v72, v73\n v_add_f32 v72, v72, v74\n v_mov_b32 v73, 0x3727c5ac\n v_fmac_f32 v73, 0x3ae38e39, v72\n v_rsq_f32 v72, v73\n s_nop 0\n v_mul_f32 v73, 0x3eae1a92, v72\n" \
    "ds_write_b32 v247, v72\n ds_read_b128 v[100:103], v249\n ds_read_b128 v[104:107], v249 offset:32\n ds_read_b128 v[108:111], v249 offset:64\n" \
    "ds_read_b128 v[112:115], v249 offset:96\n v_mul_f32 v74, -v72, v73\n .endm\n" \
    ".macro p_stl\n buffer_load_dwordx4 v[232:235], v97, s[84:87], s89 offen sc1\n s_add_u32 s88, s89, 0x40000\n buffer_load_dwordx4 v[236:239], v97, s[84:87], s88 offen sc1\n" \
    "s_add_u32 s88, s89, 0x80000\n buffer_load_dwordx4 v[240:243], v97, s[84:87], s88 offen sc1\n .endm\n" \
    ".macro p_stat\n s_cmp_lt_i32 s89, 0\n s_cbranch_scc1 8f\n s_mov_b32 s83, 0x40000\n" \
    "7:\n v_max3_u32 v75, v232, v233, v234\n v_max3_u32 v76, v235, v236, v237\n v_max3_u32 v75, v75, v238, v239\n v_max3_u32 v76, v76, v240, v241\n v_max3_u32 v75, v75, v242, v243\n" \
    "v_max_u32 v75, v75, v76\n v_cmp_eq_u32 vcc, -1, v75\n s_cbranch_vccz 9f\n s_sleep 2\n p_stl\n s_waitcnt vmcnt(0)\n s_sub_u32 s83, s83, 1\n s_cbranch_scc0 7b\n 9:\n" \
    "v_and_b32 v232, v232, v99\n v_and_b32 v234, v234, v99\n .set p_j, 0\n .rept 12\n v_lshlrev_b32 v80, 16, v[232+p_j]\n v_and_b32 v81, 0xffff0000, v[232+p_j]\n" \
    ".if p_j == 0 || p_j == 2\n v_mul_f32 v[76+p_j], v80, v80\n v_mul_f32 v[77+p_j], v81, v81\n .else\n v_fmac_f32 v[76+(p_j&2)], v80, v80\n v_fmac_f32 v[77+(p_j&2)], v81, v81\n .endif\n" \
    ".set p_j, p_j+1\n .endr\n v_add_f32 v76, v76, v77\n v_add_f32 v78, v78, v79\n s_nop 1\n v_permlane32_swap_b32 v76, v78\n s_nop 0\n v_add_f32 v76, v76, v78\n v_mov_b32 v77, v76\n s_nop 1\n" \
    "v_permlane16_swap_b32 v76, v77\n s_nop 0\n v_add_f32 v76, v76, v77\n s_nop 1\n v_add_f32_dpp v76, v76, v76 row_ror:8 row_mask:0xf bank_mask:0xf\n s_nop 1\n" \
    "v_add_f32_dpp v76, v76, v76 row_ror:4 row_mask:0xf bank_mask:0xf\n v_min_u32 v76, -2, v76\n buffer_store_dword v76, v98, s[84:87], 0 offen sc1\n s_mov_b32 s89, -1\n 8:\n .endm\n" \
    ".macro p_step lead\n .set p_x, 100+4*p_s\n .set p_b, 4*(p_s&1)\n" \
    ".rept 2\n .if p_i <= 35 && p_i <= p_s+\\lead\n p_load\n .endif\n .endr\n" \
    ".if p_s == " T_STR(STL) "\n s_cmp_lt_i32 s89, 0\n s_cbranch_scc1 6f\n p_stl\n 6:\n .set p_k, p_i\n .endif\n .if p_s == 0\n p_ready 0\n" ".endif\n" \
    "p_mm 0, 48+p_b, p_x\n p_w 48, 93, 1792\n p_mm 16, p_x, 56+p_b\n p_w 56, 94, 1792\n p_ready p_s+1\n" \
    ".if p_n == 3\n p_mm 32, 64+p_b, p_x\n p_w 64, 95, 1024\n .endif\n .set p_s, p_s+1\n .if p_s == p_k\n p_stat\n .endif\n .if p_s == " T_STR(SSA) "+1\n p_rho\n .endif\n .endm\n" \
    ".macro p_all n, lead\n .set p_n, \\n\n .set p_s, 0\n .set p_i, " T_STR(QLP) "\n .set p_k, 0\n .rept " T_STR(QL1) "-" T_STR(QLP) "\n p_load\n .endr\n .rept 35\n p_step \\lead\n .endr\n" \
    ".set p_n, 1\n p_mm 16, 240, 60\n p_mm 0, 52, 240\n .if \\n == 3\n p_mm 32, 68, 240\n .endif\n .endm\n"
#define P_CLOB "v0", "v1", "v2", "v3", "v4", "v5", "v6", "v7", "v8", "v9", "v10", "v11", "v12", "v13", "v14", "v15", "v16", "v17", "v18", "v19", "v20", "v21", "v22", "v23", "v24", \
    "v25", "v26", "v27", "v28", "v29", "v30", "v31", "v32", "v33", "v34", "v35", "v36", "v37", "v38", "v39", "v40", "v41", "v42", "v43", "v44", "v45", "v46", "v47", "v48", \
    "v49", "v50", "v51", "v52", "v53", "v54", "v55", "v56", "v57", "v58", "v59", "v60", "v61", "v62", "v63", "v64", "v65", "v66", "v67", "v68", "v69", "v70", "v71", "v72", \
    "v73", "v74", "v75", "v100", "v101", "v102", "v103", "v104", "v105", "v106", "v107", "v108", "v109", "v110", "v111", "v112", "v113", "v114", "v115", "v116", "v117", \
    "v118", "v119", "v120", "v121", "v122", "v123", "v124", "v125", "v126", "v127", "v128", "v129", "v130", "v131", "v132", "v133", "v134", "v135", "v136", "v137", "v138", \
    "v139", "v140", "v141", "v142", "v143", "v144", "v145", "v146", "v147", "v148", "v149", "v150", "v151", "v152", "v153", "v154", "v155", "v156", "v157", "v158", "v159", \
    "v160", "v161", "v162", "v163", "v164", "v165", "v166", "v167", "v168", "v169", "v170", "v171", "v172", "v173", "v174", "v175", "v176", "v177", "v178", "v179", "v180", \
    "v181", "v182", "v183", "v184", "v185", "v186", "v187", "v188", "v189", "v190", "v191", "v192", "v193", "v194", "v195", "v196", "v197", "v198", "v199", \
    "v209", "v210", "v211", "v212", "v213", "v214", "v215", "v216", "v217", "v218", "v219", "v220", "v221", "v222", \
    "v223", "v224", "v225", "v226", "v227", "v228", "v229", "v230", "v231", "v232", "v233", "v234", "v235", "v236", "v237", "v238", "v239", "v240", "v241", "v242", "v243"
template <bool LAST>
__device__ __forceinline__ unsigned qa_phase(char* ws, char* out, char* lds, int bx, int w, int& lane, unsigned when, u32x2 tag) {
    const int r = lane & 31, hh = lane >> 5;
    const int b = (bx & 4) + ((bx >> 3) & 3), h = (bx & 3) * 8 + (bx >> 5);
    const char* xh = ws + (LAST ? O_XH3 : O_XH1);
    const int kh = (w >> 2) ^ (h & 1), row0 = b * 256 + kh * 128 + (w & 3) * 16, row = row0 + (r >> 4) * 64 + (r & 15);
    const unsigned lb = (unsigned)(uintptr_t)lds;
    constexpr int SSO = (int)(O_SS - (LAST ? O_XH3 : O_XH1)) + (LAST ? 32768 : 0);
    const rsrc_t rx = mkrsrc(xh, SSO + 32768);
    const int xo = (lane >> 4) * 16384 + (2 * b + kh) * 1024 + (w & 3) * 256 + (lane & 15) * 16;
    rsrc_t rk = mkrsrc(ws + (LAST ? O_KVB : O_KVA) + (size_t)h * 8 * KVCH, 8 * (int)KVCH);
    const int a = h & 7, eo = 9 * a * (S * 16);
    rsrc_t rw = mkrsrc(ws + (LAST ? O_WO2 : O_WOH) + (unsigned)(2 * a + kh) * 41984u, 41984);
    rsrc_t ro = mkrsrc(ws + (LAST ? O_OB : O_OA), (int)(LAST ? CELLS : 2 * CELLS + PLANES)), r3 = mkrsrc(ws + O_X3, (int)CELLS);
    const char* fb = ws + O_FLG + (LAST ? 65536 : 0);
    const int rp = (r & 19) | ((r & 4) << 1) | ((r & 8) >> 1);
    const bool isv = r < 18;
    const int cslot = ((r >= 20 && r < 28) ? 4 : 2) + ((r == 18 || r == 20) ? 0 : 1);
    int own = b * (int)KVCH - lb, so;
    asm("s_lshl_b32 %0, %1, 10" : "=s"(so) : "s"(w) : "scc");
    const unsigned ko = lb + w * 1024 + hh * 512 + rp * 16, vo = lb + 8192 + w * 1280 + hh * 288 + r * 16, k2o = lb + 9344 + w * 1280 + rp * 4;
    const unsigned scw = lb + L_SC + w * 1008 + r * 4, scr = lb + L_SC + w * 1008 + 16 * hh, scl = scr + (r - 17) * 144;
    unsigned vp = lb + (isv ? 8192 + hh * 288 + r * 16 : ATT_CONST + hh * 288 + cslot * 16), vstep = isv ? (int)KVCH : 0;
    unsigned kp = lb + lane * 16, k2p = lb + 9344 + hh * 1280 + r * 4, pd = lane * 4;
    unsigned foff = (unsigned)(O_KF - O_FLG) + (LAST ? 196608 : 0) + h * 8192 + w * 1024 + ((b + 1 + lane) & 7) * 128;
    if (LAST) tag = *(const LAS u32x2*)(lds + L_CNT + 24);
    const unsigned oo = ((9 * (h >> 2) + (h & 2) * 2 + hh) * S + row) * 16, oo2 = ((36 * (h >> 2) + 32 + (h & 3)) * S + row) * 4;
    int mine = h * 1024 + b * 128;
    unsigned fl = 32768 + (hh ? r * 1024 + w * 128 : (r ^ (16 * kh)) * 1024 + b * 128), ooff = ((lane >> 4) * S + b * 256 + (h >> 3) * 64 + (w & 3) * 16 + (lane & 15)) * 16;
    rsrc_t rs = LAST ? r3 : mkrsrc(xh - 32768, (int)CELLS + 32768);
    int es = LAST ? eo : eo + 32768 - b * 2048 - ((h + 8) >> 4) * 1024 + (h & 8);
    const unsigned pa = lb + L_W + lane * 16, pb = lb + L_W + 1024 + hh * 384 + (r < 24 ? r : 23) * 16, pc = lb + L_F2 + lane * 16;
    const unsigned qo = SSO + (2 * b + kh) * 2048 + (w & 3) * 512 + (r & 15) * 32 + (r & 16);
    int redo = 0, sto = (unsigned)w < 3u ? w * 786432 + b * 2048 + h * 64 : -1;
    const unsigned sti = (lane >> 2) * 16384 + (lane & 3) * 16, stm = (w == 0 && lane < 4) ? 0xFFFF0000u : ~0u;
    const unsigned std = (lane & 28) ? 0x40000000 : SSO + b * 4096 + h * 128 + w * 4 + (lane & 3) * 32 + hh * 16;
    u32x8 ad0 = {(unsigned)xo, pa, pb, pc, qo, sti, std, stm}, ad1 = {ko, vo, k2o, scw, scl, scr, oo, oo2};
    rsrc_t rin = rx;
    asm volatile("" : "+s"(rk), "+s"(rw), "+s"(fb), "+s"(own), "+s"(so), "+s"(mine), "+s"(ro), "+s"(rs), "+s"(es), "+{s[84:87]}"(rin), "+{s89}"(sto));
    asm volatile("" : "+{v85}"(kp), "+{v86}"(k2p), "+{v87}"(vp), "+{v88}"(vstep), "+{v90}"(foff), "+{v91}"(pd), "+{v[92:99]}"(ad0), "+{v[244:251]}"(ad1), "+{v[252:253]}"(tag), "+{v254}"(fl), "+{v255}"(ooff));
    u32x4 qf; u32x2 qa, qb; u32x8 xa; unsigned xb;
    asm volatile(
        "s_mov_b32 s83, 0x4000\n 12:\n s_memrealtime vcc\n s_waitcnt lgkmcnt(0)\n v_sub_u32 v75, vcc_lo, %[when]\n v_cmp_gt_i32 vcc, 0, v75\n s_nop 3\n s_cmp_eq_u32 vcc_hi, 0\n"
        "s_cbranch_scc1 13f\n s_sleep 1\n s_sub_u32 s83, s83, 1\n s_cbranch_scc0 12b\n 13:\n"
        ".if " T_STR(QLP) "\n s_barrier\n .endif\n 1:\n .set p_i, 0\n .rept " T_STR(QLP) "\n p_load\n .endr\n s_cmp_eq_u32 %[redo], 0\n s_cbranch_scc0 14f\n"
        A_BAR0
        "14:\n ds_read_b128 v[48:51], v93\n ds_read_b128 v[56:59], v94\n"
        ".if %[last]\n s_cmp_lt_u32 %[w], 4\n s_cbranch_scc0 4f\n ds_read_b128 v[64:67], v95\n p_all 3, " T_STR(QLC) "\n s_branch 5f\n 4:\n .endif\n p_all 2, %[lead]\n 5:\n"
        "s_mov_b32 exec_lo, 0xfc0000\n s_mov_b32 exec_hi, 0xfc0000\n s_nop 7\n"
        "ds_write_b128 v248, v[16:19]\n ds_write_b128 v248, v[20:23] offset:32\n ds_write_b128 v248, v[24:27] offset:64\n ds_write_b128 v248, v[28:31] offset:96\n s_mov_b64 exec, -1\n"
        "ds_read2_b32 v[52:53], v247 offset0:108 offset1:144\n ds_read2_b32 v[50:51], v247 offset0:36 offset1:72\n .if %[last]\n ds_read2_b32 v[54:55], v247 offset0:180 offset1:216\n .endif\n"
        "p_cvt 56, 16, 100, 1\n"
        "v_cmp_u_f32 vcc, v0, v0\n s_cbranch_vccz 3f\n v_mov_b32 v75, 0\n .set p_i, 116\n .rept 64\n v_max3_u32 v75, v75, v[p_i], v[p_i+1]\n .set p_i, p_i+2\n .endr\n"
        "v_cmp_eq_u32 vcc, -1, v75\n s_cbranch_vccz 3f\n"
        "2:\n s_waitcnt vmcnt(0) lgkmcnt(0)\n s_sleep 2\n s_add_u32 %[redo], %[redo], 1\n s_bitcmp0_b32 %[redo], 18\n s_cbranch_scc1 1b\n 3:\n"
        "s_mov_b32 exec_lo, 0x3ffff\n s_mov_b32 exec_hi, 0x3ffff\n buffer_store_dwordx4 v[56:59], v245, %[rk], %[own] offen sc1\n ds_write_b128 v245, v[56:59]\n"
        "p_cvt 60, 24, 108, 1\n buffer_store_dwordx4 v[60:63], v245, %[rk], %[own] offen offset:576 sc1\n ds_write_b128 v245, v[60:63] offset:576\n s_mov_b64 exec, -1\n"
        "p_cvt 64, 8, 72, 0\n buffer_store_dwordx4 v[64:67], v244, %[rk], %[own] offen sc1\n ds_write_b128 v244, v[64:67]\n"
        "s_waitcnt lgkmcnt(3)\n v_mul_f32 v68, v52, v72\n v_mul_f32 v69, v53, v72\n s_mov_b32 exec_lo, 0\n v_cvt_pk_bf16_f32 v68, v68, v69\n"
        "buffer_store_dword v68, v246, %[rk], %[own] offen sc1\n ds_write_b32 v246, v68\n s_mov_b64 exec, -1\n"
        "s_cmp_lt_u32 %[w], 4\n s_cbranch_scc1 10f\n" A_BAR "10:\n"
        "v_mul_f32 v70, v0, v8\n v_mul_f32 v71, v1, v9\n v_fmac_f32 v70, v2, v10\n v_fmac_f32 v71, v3, v11\n v_fmac_f32 v70, v4, v12\n v_fmac_f32 v71, v5, v13\n"
        "v_fmac_f32 v70, v6, v14\n v_fmac_f32 v71, v7, v15\n v_add_f32 v70, v70, v71\n v_mov_b32 v71, v70\n v_mul_f32 v80, v50, v73\n v_mul_f32 v81, v51, v73\n"
        "v_permlane32_swap_b32 v70, v71\n v_cvt_pk_bf16_f32 v80, v80, v81\n v_add_f32 v70, v70, v71\n v_fmac_f32 v70, v51, v53\n v_fmac_f32 v70, v50, v52\n v_mul_f32 v70, v70, v74\n"
        "v_cvt_pk_bf16_f32 v81, v70, 0\n p_cvt 76, 0, 73, 0\n v_mov_b64 v[82:83], 0\n s_mov_b32 exec_lo, 0\n v_mov_b64 v[82:83], v[80:81]\n v_mov_b64 v[80:81], 0\n s_mov_b64 exec, -1\n"
        ".if %[last]\n v_cvt_pk_bf16_f32 v208, v54, v55\n .endif\n"
        "s_cbranch_scc0 11f\n"
        ".if %[last]\n .set p_j, 0\n .rept 8\n v_cvt_pk_bf16_f32 v[200+p_j], v[32+2*p_j], v[33+2*p_j]\n .set p_j, p_j+1\n .endr\n .endif\n"
        A_TELL A_BAR "11:\n"
        : "={v[76:79]}"(qf), "={v[80:81]}"(qa), "={v[82:83]}"(qb), "={v[200:207]}"(xa), "={v208}"(xb), [redo] "+s"(redo), "+{s89}"(sto)
        : "{v90}"(foff), "{v[92:99]}"(ad0), "{v[244:251]}"(ad1), "{v[252:253]}"(tag), "{s[84:87]}"(rin), [fb] "s"(fb), [rk] "s"(rk), [own] "s"(own), [w] "s"(w), [when] "v"(when), [last] "n"(LAST), [lead] "n"(LAST ? QLB : QLA)
        : "memory", "scc", "vcc", "s83", "s88", P_CLOB);
    f32x16 acc;
    u32x32 rf, w0, w1, w2, w3, w4; u32x4 rg, w5; u32x12 res; unsigned l16;
    {
        int tmp;
        asm volatile(
            "s_movk_i32 s84, 0x7f\n s_add_u32 s86, %[so], %[lb]\n s_add_u32 s86, s86, 0x4800\n s_lshr_b32 s87, %[so], 2\n s_add_u32 s87, s87, 0x2000\n s_lshr_b32 s89, %[mine], 7\n"
            "s_add_u32 s89, s89, 1\n s_mov_b32 s88, 0x200000\n" A_PRI(0) A_SG(0)
            A_PRIME0 A_SG(1) A_S0F A_SG(2) A_PRI(1) A_S1(A_LK(1)) A_SG(3) A_PRI(2) A_S2(A_LK(2)) A_SG(4) A_PRI(3) A_S3(A_LK(3)) A_PRI(4) A_S4(A_LK(4)) A_PRI(5) A_S5(A_LK(5)) A_PRI(6) A_S6(A_LK(6))
            A_PRI(7) A_STEP("32", , , A_R128("72", "v87", "9536") A_NEXT)
            "31:\n" A_SEE "s_and_b32 %[tmp], s85, 2\n s_xor_b32 %[tmp], %[tmp], s84\n s_and_b32 %[tmp], %[tmp], 3\n s_cbranch_scc0 32f\n" A_REQ "s_sleep 1\n" A_LOOK
            "s_sub_u32 s88, s88, 1\n s_cbranch_scc0 31b\n 32:\n"
            A_BAR
            A_REQ "s_bcnt1_i32_b32 s83, s84\n" A_PR(3) A_PRIME A_S0(A_REQ) A_S1(A_REQ) A_PR(2) A_S2(A_REQ) A_PR(1) A_S3(A_REQ "s_cmp_eq_u32 s84, s85\n s_cbranch_scc1 33f\n" A_LOOK "33:\n")
            A_PR(0) A_S4("s_bcnt1_i32_b32 %[tmp], s84\n s_sub_u32 %[tmp], s83, %[tmp]\n s_waitcnt vmcnt(12)\n s_cmp_eq_u32 %[tmp], 4\n s_cbranch_scc1 34f\n s_waitcnt vmcnt(9)\n"
                         "s_cmp_eq_u32 %[tmp], 3\n s_cbranch_scc1 34f\n s_waitcnt vmcnt(0)\n 34:\n")
            A_BAR A_PR(3) A_S567(A_REQ A_X3(5), "s_cmp_eq_u32 s84, 0\n s_cbranch_scc1 35f\n" A_SEE A_REQ A_REQ A_LOOK "35:\n" A_X3(6), A_X3(7))
            A_S0("s_cmp_eq_u32 s84, 0\n s_cbranch_scc1 37f\n 36:\n" A_SEE "38:\n" A_REQ "s_cmp_lg_u32 s85, 0\n s_cbranch_scc1 38b\n s_cmp_eq_u32 s84, 0\n s_cbranch_scc1 37f\n"
                 "s_sleep 1\n" A_LOOK "s_sub_u32 s88, s88, 1\n s_cbranch_scc0 36b\n 37:\n")
            A_PR(2) A_S1() A_S2() A_PR(1) A_S3() A_PR(0) A_S4("s_waitcnt vmcnt(0)\n")
            A_BAR "s_memtime s[84:85]\n s_memrealtime s[86:87]\n" A_PR(3)
            A_S567(A_WL("0") A_WL("1") A_W3("60", "v_mov_b64 v[250:251], -1\n v_mov_b64 v[252:253], -1\n s_and_b32 %[tmp], %[mine], 0xfc00\n s_add_u32 %[tmp], %[tmp], %[mine]\n"
                       "s_lshl_b32 %[tmp], %[tmp], 5\n" A_PZ(0) A_PZ(1024) A_PZ(2048) A_PZ(3072) "s_add_u32 %[tmp], %[tmp], 0x8000\n" A_PZ(0) A_PZ(1024) A_PZ(2048) A_PZ(3072)
                       "s_lshl_b32 %[tmp], %[mine], 3\n s_add_u32 %[tmp], %[tmp], 0x200000\n" A_PZ(0)),
                   A_WL("2") A_WL("3"),
                   A_WL("4") A_WL("5"))
            A_CHUNK("6", ".if %[nx] == 0\n" A_W3("61", "s_lshr_b32 %[tmp], %[mine], 10\n s_mul_i32 %[tmp], %[tmp], 73\n s_lshr_b32 %[tmp], %[tmp], 4\n s_lshl_b32 %[tmp], %[tmp], 10\n"
                       "s_and_b32 s88, %[mine], 0x380\n s_or_b32 %[tmp], %[tmp], s88\n s_lshl_b32 %[tmp], %[tmp], 4\n s_add_u32 %[tmp], %[tmp], 72*0x8000\n"
                       A_PZ2 A_PZ2 A_PZ2 A_PZ2 A_PZ2) ".endif\n")
            A_CHUNK("14", ".if %[nx] == 0\n" A_W3("62", "s_mul_i32 %[tmp], %[mine], 72\n s_add_u32 %[tmp], %[tmp], 145*0x8000\n" A_PZ3 A_PZ3 A_PZ3) ".endif\n")
            A_PR(2) A_CHUNK("22", )
            "s_memtime s[88:89]\n s_memrealtime vcc\n"
            A_CHUNK("30", "s_cmp_eq_u32 %[w], 3\n s_cbranch_scc0 67f\n s_waitcnt vmcnt(8)\n s_mov_b64 exec, 1\n s_sub_u32 %[tmp], s88, s84\n v_cvt_f32_u32 v250, %[tmp]\n"
                "s_sub_u32 %[tmp], vcc_lo, s86\n v_rcp_f32 v250, v250\n v_cvt_f32_u32 v251, %[tmp]\n s_add_u32 %[tmp], vcc_lo, " T_STR(T_D) "\n"
                "v_mul_f32 v251, " T_STR(T_R) ", v251\n v_mul_f32 v250, v250, v251\n v_cvt_u32_f32 v250, v250\n v_add_u32 v250, %[tmp], v250\n v_or_b32 v250, 1, v250\n"
                "s_add_u32 %[tmp], %[mine], 0x8000\n v_mov_b32 v251, %[tmp]\n global_store_dword v251, v250, %[fb] sc1\n s_mov_b64 exec, -1\n 67:\n")
            A_PR(1) A_S0(A_WL("38")) A_S1(A_WL("39")) A_S2(A_WL("40"))
            A_S3("global_load_dword v254, v254, %[fb] sc1\n") A_S4()
            A_S5("v_lshrrev_b32 v88, 1+%[nx]*11/2, v255\n v_and_b32 v88, 0x4000>>(%[nx]*11/2), v88\n v_and_b32 v85, 0x7fff, v255\n v_add_u32 v88, v88, v85\n") A_PR(0)
            A_S6("s_waitcnt vmcnt(0)\n s_cmp_lt_u32 %[w], 4\n s_cbranch_scc0 68f\n .if %[nx]\n"
                 "buffer_load_dwordx4 v[56:59], v255, %[rs], %[eo] offen sc1\n s_add_u32 %[tmp], %[eo], 0x20000\n"
                 "buffer_load_dwordx4 v[64:67], v255, %[rs], %[tmp] offen sc1\n v_and_b32 v60, 8, v88\n v_lshrrev_b32 v61, 2, v85\n v_lshl_or_b32 v60, v60, 11, v61\n s_add_u32 %[tmp], %[eo], 0x42000\n"
                 "buffer_load_dword v61, v60, %[rs], %[tmp] offen sc1\n s_add_u32 %[tmp], %[eo], 0x40000\n buffer_load_dword v60, v60, %[rs], %[tmp] offen sc1\n .else\n"
                 "buffer_load_dwordx2 v[56:57], v255, %[rs], %[eo] offen sc1\n s_add_u32 %[tmp], %[eo], 0x4000\n"
                 "buffer_load_dwordx2 v[58:59], v255, %[rs], %[tmp] offen sc1\n s_add_u32 %[tmp], %[eo], 0x20000\n"
                 "buffer_load_dwordx2 v[64:65], v255, %[rs], %[tmp] offen sc1\n s_add_u32 %[tmp], %[eo], 0x24000\n"
                 "buffer_load_dwordx2 v[66:67], v255, %[rs], %[tmp] offen sc1\n s_add_u32 %[tmp], %[eo], 0x40000\n"
                 "buffer_load_dwordx2 v[60:61], v88, %[rs], %[tmp] offen sc1\n .endif\n s_branch 69f\n"
                 "68:\n v_mov_b64 v[56:57], 0\n v_mov_b64 v[58:59], 0\n v_mov_b64 v[60:61], 0\n v_mov_b64 v[64:65], 0\n v_mov_b64 v[66:67], 0\n 69:\n")
            A_STEP("32", , , A_R128("72", "v87", "9536"))
            "s_waitcnt lgkmcnt(0)\n" A_PV("72", "48")
            T_MK(16) T_MK(20) T_MK(24) T_MK(28) T_MK(32) T_MK(36) T_MK(40) T_MK(44)
            "v_sub_u32 v86, v255, v85\n v_lshrrev_b32 v85, 2, v85\n v_or_b32 v85, v85, v86\n v_and_b32 v87, 0x1f0, v89\n v_and_b32 v86, 0x200, v89\n"
            "v_lshl_or_b32 v86, v86, 6, v87\n v_lshrrev_b32 v87, 2, v87\n" T_MK(48)
            : "={v[0:15]}"(acc), "={v[16:47]}"(rf), "={v[48:51]}"(rg), "={v[56:67]}"(res), "+{v85}"(kp), "+{v86}"(k2p), "+{v87}"(vp), "+{v88}"(vstep), "={v89}"(l16),
              "={v[90:121]}"(w0), "={v[122:153]}"(w1), "={v[154:185]}"(w2), "={v[186:217]}"(w3), "={v[218:249]}"(w4), "={v[250:253]}"(w5), "+{v254}"(fl),
              [tmp] "=&s"(tmp)
            : "{v[76:79]}"(qf), "{v[80:81]}"(qa), "{v[82:83]}"(qb), "{v90}"(foff), "{v91}"(pd), "{v[200:207]}"(xa), "{v208}"(xb), "{v[244:251]}"(ad1), "{v[252:253]}"(tag), "{v255}"(ooff), [rk] "s"(rk), [rw] "s"(rw), [rs] "s"(rs),
              [fb] "s"(fb), [w] "s"(w), [lb] "s"(lb), [so] "s"(so), [mine] "s"(mine), [eo] "s"(es), [ro] "s"(ro), [nx] "n"(LAST ? 2 : 0)
            : "memory", "scc", "vcc", "s83", "s84", "s85", "s86", "s87", "s88", "s89", "v52", "v53", "v54", "v55", "v68", "v69", "v70", "v71", "v72", "v73", "v74", "v75", "v84");
    }
    const unsigned loff = kp, om = k2p, ol = vp, eoff = vstep;
    const bool up = l16 >= 512;
    float l = up ? acc[8] : acc[10];
    const f32x2 sm = (f32x2{acc[0], acc[1]} + f32x2{acc[2], acc[3]}) + (f32x2{acc[4], acc[5]} + f32x2{acc[6], acc[7]}) + f32x2{acc[8], acc[9]};
    const bool ok = l > 1e-12f && l < 1e30f && fabsf(sm[0] + sm[1]) < 3e38f;
    if (__builtin_expect(!__all(ok), 0)) {
        acc = f32x16{};
        float m = -3.0e38f;
        const unsigned cv = (ol == 72 || ol == 80) ? 0x3F803F80u : 0u, va = 8192 + 4 * ol + (up ? 288 : 0);
#pragma nounroll
        for (int t = 0; t < 64; ++t) {
            const int co = (t >> 3) * (int)KVCH, tt = t & 7;
            const unsigned k2 = __builtin_amdgcn_raw_buffer_load_b32(rk, ol + (up ? 1280 : 0), co + 9344 + (tt >> 1) * 2560, 16);
            const u32x4 k1 = __builtin_amdgcn_raw_buffer_load_b128(rk, l16, co + tt * 1024, 16);
            u32x4 v0 = {cv, cv, cv, cv}, v1 = v0;
            if (ol < 72) {
                v0 = __builtin_amdgcn_raw_buffer_load_b128(rk, va, co + tt * 1280, 16);
                v1 = __builtin_amdgcn_raw_buffer_load_b128(rk, va, co + tt * 1280 + 576, 16);
            }
            f32x16 sA = MF32(asb(k1), asb(qf), f32x16{});
            sA = __builtin_amdgcn_mfma_f32_32x32x8bf16_1k(__builtin_bit_cast(s16x4, u32x2{k2, 0u}), __builtin_bit_cast(s16x4, u32x2{(tt & 1) ? qb[0] : qa[0], 0u}), sA, 0, 0, 0);
            const float mn = fmaxf(m, max16(sA)), al = EX(m - mn);
            m = mn;
#pragma unroll
            for (int i = 0; i < 16; ++i) acc[i] *= al;
            f32x8 p0, p1;
#pragma unroll
            for (int i = 0; i < 8; ++i) { p0[i] = EX(sA[i] - mn); p1[i] = EX(sA[8 + i] - mn); }
            acc = __builtin_amdgcn_smfmac_f32_32x32x32_bf16(asb(v0), __builtin_bit_cast(bf16x16, p0), acc, (int)0xDDDDDDDDu, 0, 0);
            acc = __builtin_amdgcn_smfmac_f32_32x32x32_bf16(asb(v1), __builtin_bit_cast(bf16x16, p1), acc, (int)0xDDDDDDDDu, 0, 0);
        }
        l = up ? acc[8] : acc[10];
        asm volatile(T_MK(16) T_MK(20) T_MK(24) T_MK(28) T_MK(32) T_MK(36) T_MK(40) T_MK(44) : "={v[16:47]}"(rf), "+v"(l));
    }
    if (__builtin_expect(!__all(fl != 0u), 0)) {
#pragma nounroll
        for (int spin = 0; spin < 0x200000; ++spin) {
            fl = __hip_atomic_load((gu32*)(fb + 32768 + (up ? ol * 256 + w * 128 : ((ol * 256) ^ (16384 * kh)) + b * 128)), __ATOMIC_RELAXED, __HIP_MEMORY_SCOPE_AGENT);
            if (__all(fl != 0u)) break;
            __builtin_amdgcn_s_sleep(1);
        }
        if (LAST && w < 4)
            asm volatile("buffer_load_dwordx4 v[56:59], %1, %3, %4 offen sc1\n buffer_load_dwordx4 v[64:67], %1, %3, %5 offen sc1\n buffer_load_dword v60, %2, %3, %6 offen sc1\n buffer_load_dword v61, %2, %3, %7 offen sc1"
                         : "+{v[56:67]}"(res) : "v"(ooff), "v"((eoff & 8) * 2048 + (eoff >> 4) * 4), "s"(rs), "s"(es), "s"(es + 0x20000), "s"(es + 0x40000), "s"(es + 0x42000));
    }
    const float il = __builtin_amdgcn_rcpf(l);
    __builtin_amdgcn_raw_buffer_store_b128(u32x4{min(pk(acc[0] * il, acc[1] * il), MK), min(pk(acc[2] * il, acc[3] * il), MK), min(pk(acc[4] * il, acc[5] * il), MK),
                                                 min(pk(acc[6] * il, acc[7] * il), MK)}, ro, om + (om & 256) * 3, (2 * h * S + row0) * 16, 16);
    if (!up) __builtin_amdgcn_raw_buffer_store_b32(min(pk(acc[8] * il, acc[9] * il), MK), ro, ol + (ol & 64) * 3, ((256 + h) * S + row0) * 4, 16);
    asm volatile("s_waitcnt vmcnt(2)" : "+{v[56:67]}"(res));
    f32x4 oc[5];
    {
        int t, go, seen = 0, todo = 0x15555, spin = 0x100000; unsigned long long ret;
        asm volatile(
            "s_call_b64 %[ret], 80f\n s_branch 65f\n"
            "79:\n s_cmp_lg_u32 %[todo], 0\n s_cbranch_scc1 80f\n s_cmp_eq_u32 %[seen], %[go]\n s_cselect_b32 %[todo], %[go], 0\n s_cselect_b32 %[seen], 0, %[go]\n"
            "s_waitcnt vmcnt(0)\n s_cbranch_scc0 83f\n"
            "80:\n s_memrealtime vcc\n s_sub_u32 %[spin], %[spin], 1\n s_cbranch_scc1 89f\n s_waitcnt lgkmcnt(0)\n v_sub_u32 v76, vcc_lo, v254\n v_cmp_le_i32 vcc, 0, v76\n"
            "s_and_b32 %[t], vcc_lo, 0xffff\n s_lshr_b32 %[go], %[t], 1\n s_and_b32 %[go], %[go], %[t]\n s_add_u32 %[t], %[t], 1\n s_and_b32 %[t], %[t], 0x10000\n"
            "s_or_b32 %[go], %[go], %[t]\n s_and_b32 %[go], %[go], %[todo]\n s_cbranch_scc0 83f\n s_andn2_b32 %[todo], %[todo], %[go]\n"
            T_GO(0) T_GO(1) T_GO(2) T_GO(3) T_GO(4) T_GO(5) T_GO(6) T_GO(7) "s_bitcmp1_b32 %[go], 16\n s_cbranch_scc0 83f\n" T_L1(0) T_L1(1) T_L1(2) T_L1(3)
            "83:\n s_setpc_b64 %[ret]\n"
            "65:\n" T_UP("68", "60") T_UP("70", "61") T_UP("52", "56") T_UP("54", "57") T_UP("56", "58") T_UP("58", "59")
            T_UP("60", "64") T_UP("62", "65") T_UP("64", "66") T_UP("66", "67")
            T_CHK(0, 11) T_STEP(0, "v[68:71]", "v[68:71]", 1, 10) T_STEP(1, "v[72:75]", "0", 2, 9) T_STEP(2, "v[68:71]", "v[68:71]", 3, 8)
            T_STEP(3, "v[72:75]", "v[72:75]", 4, 7) T_STEP(4, "v[68:71]", "v[68:71]", 5, 6) T_STEP(5, "v[72:75]", "v[72:75]", 6, 5)
            T_STEP(6, "v[68:71]", "v[68:71]", 7, 4) T_STEP(7, "v[72:75]", "v[72:75]", 8, 0)
            T_M(8, 0) "v_permlane32_swap_b32 v72, v16\n v_permlane32_swap_b32 v73, v17\n" T_M(8, 1) "v_permlane32_swap_b32 v74, v18\n v_permlane32_swap_b32 v75, v19\n"
            T_M(8, 2) "v_pk_add_f32 v[68:69], v[68:69], v[16:17]\n v_pk_add_f32 v[70:71], v[70:71], v[18:19]\n" T_M(8, 3) "s_nop 0\n" T_H(8, "v[68:71]", "v[68:71]")
            "89:\n" "s_nop 7\n"
            : "={v[52:55]}"(oc[0]), "={v[56:59]}"(oc[1]), "={v[60:63]}"(oc[2]), "={v[64:67]}"(oc[3]), "={v[68:71]}"(oc[4]), "+{v[16:47]}"(rf), "+{v[48:51]}"(rg),
              [t] "=&s"(t), [go] "=&s"(go), [todo] "+s"(todo), [seen] "+s"(seen), [spin] "+s"(spin), [ret] "=&s"(ret)
            : "{v[56:67]}"(res), "{v85}"(loff), "{v[90:121]}"(w0), "{v[122:153]}"(w1), "{v[154:185]}"(w2), "{v[186:217]}"(w3), "{v[218:249]}"(w4), "{v[250:253]}"(w5),
              "{v254}"(fl), "{v255}"(ooff), [ro] "s"(ro), [ko] "s"(kh * 0x100000), [lo] "s"(0x200000 + kh * 0x20000)
            : "memory", "scc", "vcc", "v72", "v73", "v74", "v75", "v76");
    }
    char* xc = lds + (w & 3) * 5120 + l16;
    const unsigned rho8 = (ooff & 0x7FFF) >> 1;
    unsigned d0 = LAST ? nrow(eoff >> 4) * 1152 + (ooff >> 15) * 16 : ooff - rho8, d1 = LAST ? nrow(eoff >> 4) * 1152 + 128 + (eoff & 8) : (ooff >> 1);
    unsigned d2 = ((rho8 >> 2) & 0x1E0) + unmu(rho8 >> 3) * 2;
    if (kh == 0) {
#pragma unroll
        for (int T = 0; T < 5; ++T) *(LAS f32x4*)(xc + T * 1024) = oc[T];
    }
    asm volatile("" : "+v"(d0), "+v"(d1));
    if (!LAST) asm volatile("s_setprio 3" : "+v"(d2));
    asm volatile("s_waitcnt lgkmcnt(0)\n\ts_barrier" ::: "memory");
    if (kh) {
        f32x4 pr[5];
#pragma unroll
        for (int T = 0; T < 5; ++T) pr[T] = *(const LAS f32x4*)(xc + T * 1024);
        SB();
#pragma unroll
        for (int T = 0; T < 5; ++T) oc[T] += pr[T];
        u32x4 o0 = {pk(oc[0][0], oc[0][1]), pk(oc[0][2], oc[0][3]), pk(oc[1][0], oc[1][1]), pk(oc[1][2], oc[1][3])};
        const u32x4 o1 = {pk(oc[2][0], oc[2][1]), pk(oc[2][2], oc[2][3]), pk(oc[3][0], oc[3][1]), pk(oc[3][2], oc[3][3])};
        const u32x2 o2 = {pk(oc[4][0], oc[4][1]), pk(oc[4][2], oc[4][3])};
        const rsrc_t rd = mkrsrc(out, (int)(LAST ? CELLS : PLANES));
        if (LAST) {
            __builtin_amdgcn_raw_buffer_store_b128(o0, rd, d0, 144 * a, 0);
            __builtin_amdgcn_raw_buffer_store_b128(o1, rd, d0 + 64, 144 * a, 0);
            if (!up) __builtin_amdgcn_raw_buffer_store_b64(o2, rd, d1, 144 * a, 0);
        } else {
            __builtin_amdgcn_raw_buffer_store_b64(u32x2{min(o0[0], MK), min(o0[1], MK)}, rd, d0, eo, 16);
            __builtin_amdgcn_raw_buffer_store_b64(u32x2{min(o0[2], MK), min(o0[3], MK)}, rd, d0, eo + 0x4000, 16);
            __builtin_amdgcn_raw_buffer_store_b64(u32x2{min(o1[0], MK), min(o1[1], MK)}, rd, d0, eo + 0x20000, 16);
            __builtin_amdgcn_raw_buffer_store_b64(u32x2{min(o1[2], MK), min(o1[3], MK)}, rd, d0, eo + 0x24000, 16);
            if (!up) __builtin_amdgcn_raw_buffer_store_b64(u32x2{min(o2[0], MK), min(o2[1], MK)}, rd, d1, eo + 0x40000, 16);
            SB();
            if (a == 0 && l16 < 256) o0[0] &= 0xFFFF0000u;
            float q = sq2(o0[1], sq2(o0[0], 0.f)), u = sq2(o1[1], sq2(o1[0], 0.f)), v = sq2(o2[1], sq2(o2[0], 0.f));
            q = sq2(o0[3], sq2(o0[2], q)); u = sq2(o1[3], sq2(o1[2], u));
            q = sum4g(q + u + (up ? 0.f : v));
            const float qn = __uint_as_float(__builtin_amdgcn_mov_dpp(__float_as_uint(q), 0xB1, 15, 15, true));
            if ((l16 & 0x310) == 0) __builtin_amdgcn_raw_buffer_store_b32(min(pk(q, qn), MK), rd, d2, (144 + (a >> 2)) * 16384 + b * 2048 + (a & 3) * 512, 16);
        }
    }
    if (!LAST) asm volatile("s_setprio 0");
    lane = l16 >> 4;
    return fl;
}

__global__ __attribute__((aligned(4096))) __launch_bounds__(512, 2) void k_main(const u16* __restrict__ x, const u16* __restrict__ wr, const u16* __restrict__ wi, const u16* __restrict__ wqkv,
                                              const u16* __restrict__ wo, char* ws, char* out) {
    __shared__ __attribute__((aligned(256))) char lds[L_END];
    asm volatile(P_MACROS ::);
    const int tid = threadIdx.x;
    int lane = tid & 63, w = __builtin_amdgcn_readfirstlane(tid >> 6), bx = blockIdx.x;
    FC K;
    const u32x2 tag = prepare(x, wr, wi, wqkv, wo, ws, lds, bx, w, lane, K);
    unsigned fl = fft_phase<false>(ws, lds, bx, w, lane, 0u, tag, wo, K);
    if (w == 0) *(LAS u32x2*)(lds + L_CNT + 24) = tag;
    fl = qa_phase<false>(ws, ws + O_XH2, lds, bx, w, lane, fl, tag);
    asm volatile("" : "+s"(ws));
    asm volatile("" : "+s"(w));
    asm volatile("" : "+s"(bx));
    fl = fft_phase<true>(ws, lds, bx, w, lane, fl, u32x2{}, wo, K);
    qa_phase<true>(ws, out, lds, bx, w, lane, fl, u32x2{});
    asm volatile("text_end_mark:");
}

extern "C" void kernel_launch(void* const* d_in, const int* in_sizes, int n_in, void* d_out, int out_size, void* d_ws, size_t ws_size, hipStream_t stream) {
    k_main<<<256, 512, 0, stream>>>((const u16*)d_in[0], (const u16*)d_in[1], (const u16*)d_in[2], (const u16*)d_in[4], (const u16*)d_in[5], (char*)d_ws, (char*)d_out);
}
```
